```python
import torch

def _softmax(x, axis=-1):
    return torch.softmax(x, dim=axis)

def _gelu(x):
    return torch.nn.functional.gelu(x, approximate='tanh')

def _silu(x):
    return torch.nn.functional.silu(x)

def _sigmoid(x):
    return torch.sigmoid(x)

def _layernorm(x, axis=-1, eps=1e-05):
    m = x.mean(dim=axis, keepdim=True)
    v = x.var(dim=axis, keepdim=True, unbiased=False)
    return (x - m) / torch.sqrt(v + eps)

def _scatter_add(tgt, idx, src, axis=0):
    return tgt.clone().index_add_(axis, idx, src)

def _segment_sum(src, seg, n):
    out = torch.zeros((n,) + src.shape[1:], dtype=src.dtype)
    return out.index_add_(0, seg, src)

def _pad(x, pw):
    flat = tuple((v for pair in reversed(pw) for v in pair))
    return torch.nn.functional.pad(x, flat)
kernel_name = 'standalone__kv_cache_quant_pre_rope_vs_post_rope__prompt'

def setup_inputs():

    _w = lambda a: (lambda t: t.to(torch.bfloat16) if t.is_floating_point() else t)(torch.as_tensor(a))
    B, Sq, Skv = (1, 2048, 2048)
    H, Dh0 = (32, 128)
    G = 32 if 32 > 0 else Dh0
    G = max(2, G // 2 * 2)
    Dh = max(G, Dh0 // G * G)
    Skv = max(Skv, Sq)
    ng = Dh // G
    bits = 4 if 4 > 1 else 4
    bits = max(2, min(8, bits))
    qmax = (1 << bits) - 1
    half = Dh // 2
    inv = 1.0 / 10000.0 ** (torch.arange(half) / max(1, half))
    ang_k = torch.arange(Skv)[:, None] * inv[None, :]
    ang_q = torch.arange(Skv - Sq, Skv)[:, None] * inv[None, :]
    kc = torch.cat([torch.cos(ang_k), torch.cos(ang_k)], dim=-1)
    ks = torch.cat([torch.sin(ang_k), torch.sin(ang_k)], dim=-1)
    qc = torch.cat([torch.cos(ang_q), torch.cos(ang_q)], dim=-1)
    qs = torch.cat([torch.sin(ang_q), torch.sin(ang_q)], dim=-1)
    qk_i = torch.randint(0, qmax + 1, (B, H, Skv, Dh)).to(torch.int32)
    qv_i = torch.randint(0, qmax + 1, (B, H, Skv, Dh)).to(torch.int32)
    ksc = torch.abs((lambda *s: torch.randn(*s, dtype=torch.bfloat16) * 0.2)(B, H, Skv, ng)) * 0.1 + 0.01
    kzp = torch.full((B, H, Skv, ng), qmax / 2.0)
    vsc = torch.abs((lambda *s: torch.randn(*s, dtype=torch.bfloat16) * 0.2)(B, H, Skv, ng)) * 0.1 + 0.01
    vzp = torch.full((B, H, Skv, ng), qmax / 2.0)
    return {'q': _w((lambda *s: torch.randn(*s, dtype=torch.bfloat16) * 6.4)(B, H, Sq, Dh) * 0.5), 'qk': _w(qk_i), 'k_scales': _w(ksc), 'k_zeros': _w(kzp), 'qv': _w(qv_i), 'v_scales': _w(vsc), 'v_zeros': _w(vzp), 'q_cos': _w(qc), 'q_sin': _w(qs), 'k_cos': _w(kc), 'k_sin': _w(ks)}

def reference(q, qk, k_scales, k_zeros, qv, v_scales, v_zeros, q_cos, q_sin, k_cos, k_sin):
    B, H, Sq, Dh = q.shape
    ng = k_scales.shape[-1]
    G = Dh // ng
    half = Dh // 2
    ks = (lambda x, n, axis: torch.repeat_interleave(x, n, dim=axis))(k_scales, G, axis=-1)
    kz = (lambda x, n, axis: torch.repeat_interleave(x, n, dim=axis))(k_zeros, G, axis=-1)
    k = ((lambda x, dt: x.to(dt))(qk, q.dtype) - kz) * ks
    vs = (lambda x, n, axis: torch.repeat_interleave(x, n, dim=axis))(v_scales, G, axis=-1)
    vz = (lambda x, n, axis: torch.repeat_interleave(x, n, dim=axis))(v_zeros, G, axis=-1)
    v = ((lambda x, dt: x.to(dt))(qv, q.dtype) - vz) * vs
    k1 = k[..., :half].clone()
    k2 = k[..., half:].clone()
    krh = torch.concatenate([-k2, k1], axis=-1)
    kr = k * k_cos[None, None, :, :] + krh * k_sin[None, None, :, :]
    q1 = q[..., :half].clone()
    q2 = q[..., half:].clone()
    qrh = torch.concatenate([-q2, q1], axis=-1)
    qr = q * q_cos[None, None, :, :] + qrh * q_sin[None, None, :, :]
    logits = torch.einsum('bhqd,bhkd->bhqk', qr, kr) * float(Dh) ** (-0.5)
    attn = _softmax(logits, axis=-1)
    out = torch.einsum('bhqk,bhkd->bhqd', attn, v)
    return out
```

```cpp
#include <hip/hip_runtime.h>
#include <hip/hip_bf16.h>
#include <cstdio>
using bf16 = __hip_bfloat16;
constexpr int   D = 128, NW = 8, QBLK = 32, KVBLK = 64;
constexpr float SCALE = 0.088388347648318440f;
constexpr float THR = 8.f;
constexpr int SDEPTH = 2;
constexpr int HQ = 32, S = 2048, SKV = 2048;
constexpr int LDQ = D, LDK = D, LDO = D;
constexpr size_t SHM_V = KVBLK * D * 2, SHM_K = KVBLK * D * 2, SHM_ATTN = 2 * SHM_V + 2 * SHM_K + NW * 64 * 4;
using bf16x8 = __attribute__((ext_vector_type(8))) short;
using s16x4  = __attribute__((ext_vector_type(4))) short;
using f32x16 = __attribute__((ext_vector_type(16))) float;
using u32x4  = __attribute__((ext_vector_type(4))) unsigned;
using i32x4  = __attribute__((ext_vector_type(4))) int;
#define KSWZ(row, colB) ((row) * 256 + ((colB) ^ (((row) & 7) << 4)))
#define SBAR() __builtin_amdgcn_sched_barrier(0)
__device__ __forceinline__ int crow(int r, int hi) { return (r & 3) + 8 * (r >> 2) + 4 * hi; }
__device__ __forceinline__ unsigned cvtpk(float lo, float hi) {
  unsigned r; asm volatile("v_cvt_pk_bf16_f32 %0, %1, %2" : "=v"(r) : "v"(lo), "v"(hi)); return r;
}
__device__ __forceinline__ bf16x8 ld8(const bf16* p) { return *reinterpret_cast<const bf16x8*>(p); }
__device__ __forceinline__ void partialSM(f32x16& p0, f32x16& p1, float& m_reg, float& mn, float& alpha) {
  constexpr float C = SCALE * 1.4426950408889634f;
  float pmax = p0[0]; for (int r = 1; r < 16; ++r) pmax = fmaxf(pmax, p0[r]); for (int r = 0; r < 16; ++r) pmax = fmaxf(pmax, p1[r]);
  { auto rr = __builtin_amdgcn_permlane32_swap(__float_as_uint(pmax), __float_as_uint(pmax), false, false);
    pmax = fmaxf(__uint_as_float(rr[0]), __uint_as_float(rr[1])); }
  if (__builtin_expect(__all(pmax - m_reg <= THR / SCALE), 1)) { mn = m_reg; alpha = 1.f; }
  else { mn = fmaxf(m_reg, pmax); alpha = __builtin_amdgcn_exp2f((m_reg - mn) * C); m_reg = mn; }
  float mnC = -mn * C;
  for (int r = 0; r < 16; ++r) p0[r] = fmaf(p0[r], C, mnC); for (int r = 0; r < 16; ++r) p1[r] = fmaf(p1[r], C, mnC);
  for (int r = 0; r < 16; ++r) p0[r] = __builtin_amdgcn_exp2f(p0[r]);
}
__device__ __forceinline__ void finishSM(f32x16& p0, f32x16& p1, float alpha, float& l_reg, bf16x8& pa0, bf16x8& pa1, bf16x8& pa2, bf16x8& pa3) {
  for (int r = 0; r < 16; ++r) p1[r] = __builtin_amdgcn_exp2f(p1[r]);
  float ps = 0; for (int r = 0; r < 16; ++r) ps += p0[r]; for (int r = 0; r < 16; ++r) ps += p1[r];
  { auto rr = __builtin_amdgcn_permlane32_swap(__float_as_uint(ps), __float_as_uint(ps), false, false);
    ps = __uint_as_float(rr[0]) + __uint_as_float(rr[1]); }
  l_reg = l_reg * alpha + ps;
#define PK4(P, BASE, OUT) do { unsigned a0 = cvtpk(P[BASE + 0], P[BASE + 1]), a1 = cvtpk(P[BASE + 2], P[BASE + 3]);   \
    unsigned b0 = cvtpk(P[BASE + 4], P[BASE + 5]), b1 = cvtpk(P[BASE + 6], P[BASE + 7]);                              \
    auto r0 = __builtin_amdgcn_permlane32_swap(a0, b0, false, false); auto r1 = __builtin_amdgcn_permlane32_swap(a1, b1, false, false); \
    u32x4 w = {r0[0], r1[0], r0[1], r1[1]}; OUT = *reinterpret_cast<bf16x8*>(&w); } while (0)
  PK4(p0, 0, pa0); PK4(p0, 8, pa1); PK4(p1, 0, pa2); PK4(p1, 8, pa3);
#undef PK4
}
__device__ __forceinline__ void qkt(f32x16& p0, f32x16& p1, const bf16* Ks, const bf16x8* qr, int r32, int hi) {
  p0 = f32x16{}; p1 = f32x16{};
  for (int d0 = 0; d0 < 8; ++d0) { int cb = (d0 * 16 + hi * 8) * 2;
    bf16x8 b0 = *reinterpret_cast<const bf16x8*>((const char*)Ks + KSWZ(r32, cb));
    bf16x8 b1 = *reinterpret_cast<const bf16x8*>((const char*)Ks + KSWZ(32 + r32, cb));
    p0 = __builtin_amdgcn_mfma_f32_32x32x16_bf16(b0, qr[d0], p0, 0, 0, 0);
    p1 = __builtin_amdgcn_mfma_f32_32x32x16_bf16(b1, qr[d0], p1, 0, 0, 0); }
}
__device__ __forceinline__ int v_st(int k, int c) { const int kk = (k & ~0xC) | ((k & 4) << 1) | ((k & 8) >> 1); return ((kk >> 3) * 4 + (c >> 5)) * 512 + ((kk & 7) * 32 + (c & 31)) * 2; }
__device__ __forceinline__ int v_rd_base(int lane) { return ((lane & 3) << 3) | (((lane >> 2) & 3) << 6) | (((lane >> 4) & 1) << 5) | (((lane >> 5) & 1) << 8); }
constexpr int v_rd_off(int d0, int ks, int half) { return d0 * 512 + ks * 4096 + half * 2048; }
template <int OFF> __device__ __forceinline__ s16x4 tr_read(int vb) {
  s16x4 r; asm volatile("ds_read_b64_tr_b16 %0, %1 offset:%2" : "=&v"(r) : "v"(vb), "i"(OFF) : "memory"); return r;
}
template <int D0> __device__ __forceinline__ void pv_one(f32x16& od, int vb, bf16x8 pa0, bf16x8 pa1, bf16x8 pa2, bf16x8 pa3) {
  const s16x4 l0 = tr_read<v_rd_off(D0, 0, 0)>(vb), h0 = tr_read<v_rd_off(D0, 0, 1)>(vb), l1 = tr_read<v_rd_off(D0, 1, 0)>(vb), h1 = tr_read<v_rd_off(D0, 1, 1)>(vb);
  const s16x4 l2 = tr_read<v_rd_off(D0, 2, 0)>(vb), h2 = tr_read<v_rd_off(D0, 2, 1)>(vb), l3 = tr_read<v_rd_off(D0, 3, 0)>(vb), h3 = tr_read<v_rd_off(D0, 3, 1)>(vb);
  asm volatile("s_waitcnt lgkmcnt(0)" ::: "memory"); SBAR();
#define PK(L, H) (bf16x8){L[0], L[1], L[2], L[3], H[0], H[1], H[2], H[3]}
  od = __builtin_amdgcn_mfma_f32_32x32x16_bf16(pa0, PK(l0, h0), od, 0, 0, 0);
  od = __builtin_amdgcn_mfma_f32_32x32x16_bf16(pa1, PK(l1, h1), od, 0, 0, 0);
  od = __builtin_amdgcn_mfma_f32_32x32x16_bf16(pa2, PK(l2, h2), od, 0, 0, 0);
  od = __builtin_amdgcn_mfma_f32_32x32x16_bf16(pa3, PK(l3, h3), od, 0, 0, 0);
#undef PK
}
__device__ __forceinline__ void pv_d0(f32x16* o, int vb, bf16x8 pa0, bf16x8 pa1, bf16x8 pa2, bf16x8 pa3) {
  pv_one<0>(o[0], vb, pa0, pa1, pa2, pa3); pv_one<1>(o[1], vb, pa0, pa1, pa2, pa3); pv_one<2>(o[2], vb, pa0, pa1, pa2, pa3); pv_one<3>(o[3], vb, pa0, pa1, pa2, pa3);
}
__device__ __forceinline__ void attn_dense_body(const bf16* __restrict__ Qb, const bf16* __restrict__ Kh, const bf16* __restrict__ Vh,
                                                bf16* __restrict__ Ob, int seq, char* lds) {
  const int tid = threadIdx.x, wid = tid >> 6, lane = tid & 63, r32 = lane & 31, hi = lane >> 5;
  bf16* V_lds = (bf16*)lds; bf16* K_lds = (bf16*)(lds + 2 * SHM_V);
  float* ws = (float*)(lds + 2 * SHM_V + 2 * SHM_K) + wid * 64; float* li_l = ws; float* al_l = ws + 32;
  float m_reg = -1e30f, l_reg = 0; f32x16 o[4] = {}; bf16x8 qr[8];
  const bf16* Qw = Qb + (long)(wid * QBLK + r32) * LDQ + hi * 8;
#pragma unroll
  for (int d0 = 0; d0 < 8; ++d0) qr[d0] = ld8(Qw + d0 * 16);
  const int sr = tid >> 4, sc = (tid & 15) * 8, vst0 = v_st(sr, sc), vst1 = v_st(32 + sr, sc);
  const int vb0 = (int)(uintptr_t)V_lds + v_rd_base(lane);
  struct { bf16x8 vs0, vs1, ks0, ks1; } sr_[SDEPTH];
#define SLOAD(i, k0) do { sr_[i].vs0 = ld8(&Vh[(long)((k0) + sr) * LDK + sc]); sr_[i].vs1 = ld8(&Vh[(long)((k0) + 32 + sr) * LDK + sc]); \
    sr_[i].ks0 = ld8(&Kh[(long)((k0) + sr) * LDK + sc]); sr_[i].ks1 = ld8(&Kh[(long)((k0) + 32 + sr) * LDK + sc]); } while (0)
#define SWRITE(b, i) do { *(bf16x8*)((char*)V_lds + (b) * SHM_V + vst0) = (sr_[i].vs0);          \
    *(bf16x8*)((char*)V_lds + (b) * SHM_V + vst1) = (sr_[i].vs1); int kc = sc * 2;               \
    *(bf16x8*)((char*)K_lds + (b) * SHM_K + KSWZ(sr, kc)) = (sr_[i].ks0);                       \
    *(bf16x8*)((char*)K_lds + (b) * SHM_K + KSWZ(32 + sr, kc)) = (sr_[i].ks1); } while (0)
#define SWAIT() do { asm volatile("s_waitcnt vmcnt(4)" ::: "memory"); } while (0)
#define RESC(a) do { if (__any((a) < 1.f)) { if (hi == 0) al_l[r32] = (a); asm volatile("s_waitcnt lgkmcnt(0)" ::: "memory"); \
    for (int d = 0; d < 4; ++d) for (int r = 0; r < 16; ++r) o[d][r] *= al_l[crow(r, hi)]; } } while (0)
  f32x16 pA0, pA1, pB0, pB1; float mnA, mnB, alA, alB; bf16x8 pa0, pa1, pa2, pa3; const int NT = seq / KVBLK;
  constexpr int SE = 0, SO = SDEPTH - 1;
  SLOAD(SE, 0); asm volatile("s_waitcnt vmcnt(0)" ::: "memory"); SWRITE(0, SE); __syncthreads();
  qkt(pA0, pA1, K_lds, qr, r32, hi); partialSM(pA0, pA1, m_reg, mnA, alA);
  SLOAD(SO, KVBLK); if (2 < NT) SLOAD(SE, 2 * KVBLK);
  SWAIT(); SWRITE(1, SO); __syncthreads();
  for (int j = 1; j + 1 < NT; j += 2) {
    SBAR(); qkt(pB0, pB1, (bf16*)((char*)K_lds + SHM_K), qr, r32, hi);
    finishSM(pA0, pA1, alA, l_reg, pa0, pa1, pa2, pa3); SBAR();
    SLOAD(SO, (j + SDEPTH) * KVBLK); SBAR();
    pv_d0(o, vb0, pa0, pa1, pa2, pa3); partialSM(pB0, pB1, m_reg, mnB, alB);
    __syncthreads(); SWAIT(); SWRITE(0, SE);
    RESC(alB); __syncthreads();
    SBAR(); qkt(pA0, pA1, K_lds, qr, r32, hi);
    finishSM(pB0, pB1, alB, l_reg, pa0, pa1, pa2, pa3); SBAR();
    if (j + 3 < NT) SLOAD(SE, (j + 1 + SDEPTH) * KVBLK); SBAR();
    pv_d0(o, vb0 + (int)SHM_V, pa0, pa1, pa2, pa3); partialSM(pA0, pA1, m_reg, mnA, alA);
    __syncthreads(); SWAIT(); SWRITE(1, SO);
    RESC(alA); __syncthreads();
  }
  SBAR(); qkt(pB0, pB1, (bf16*)((char*)K_lds + SHM_K), qr, r32, hi);
  finishSM(pA0, pA1, alA, l_reg, pa0, pa1, pa2, pa3); SBAR();
  pv_d0(o, vb0, pa0, pa1, pa2, pa3); partialSM(pB0, pB1, m_reg, mnB, alB);
  __syncthreads(); RESC(alB);
  finishSM(pB0, pB1, alB, l_reg, pa0, pa1, pa2, pa3); SBAR();
  pv_d0(o, vb0 + (int)SHM_V, pa0, pa1, pa2, pa3);
  if (hi == 0) li_l[r32] = l_reg; asm volatile("s_waitcnt lgkmcnt(0)" ::: "memory");
  float rli[16];
#pragma unroll
  for (int r = 0; r < 16; ++r) rli[r] = __builtin_amdgcn_rcpf(li_l[crow(r, hi)]);
  bf16* Ow = Ob + (long)(wid * QBLK) * LDO;
#pragma unroll
  for (int r = 0; r < 16; ++r) { int orow = crow(r, hi);
    for (int d0 = 0; d0 < 4; ++d0) Ow[(long)orow * LDO + d0 * 32 + r32] = __float2bfloat16(o[d0][r] * rli[r]); }
}
__global__ __launch_bounds__(64 * NW, 1)
void attn_fwd_dense(const bf16* __restrict__ Q, const bf16* __restrict__ K, const bf16* __restrict__ V, bf16* __restrict__ O) {
  extern __shared__ __attribute__((aligned(16))) char shm[];
  const int bid = blockIdx.x; const int xcd = bid & 7, idx = bid >> 3;
  const long h = xcd * 4 + (idx >> 3), i0 = (long)(idx & 7) * (QBLK * NW);
  const long q0 = (h * S + i0) * D, k0 = h * SKV * (long)D;
  attn_dense_body(Q + q0, K + k0, V + k0, O + q0, SKV, shm);
}
__device__ __forceinline__ float bf2f(unsigned short u) { return __uint_as_float((unsigned)u << 16); }
__device__ __forceinline__ float rbf(float x) { return __bfloat162float(__float2bfloat16(x)); }
__global__ __launch_bounds__(256) void prepass(const bf16* __restrict__ q, const int* __restrict__ qk, const bf16* __restrict__ ksc, const bf16* __restrict__ kzp,
    const int* __restrict__ qv, const bf16* __restrict__ vsc, const bf16* __restrict__ vzp, const bf16* __restrict__ qc, const bf16* __restrict__ qs,
    const bf16* __restrict__ kc, const bf16* __restrict__ ks, bf16* __restrict__ Qo, bf16* __restrict__ Ko, bf16* __restrict__ Vo) {
  const long i = blockIdx.x * 256L + threadIdx.x;
  const long row = i >> 4; const int d = (int)(i & 15) * 4; const int s = (int)(row & (S - 1));
  const i32x4 k1 = *(const i32x4*)(qk + row * D + d), k2 = *(const i32x4*)(qk + row * D + d + 64);
  const i32x4 v1 = *(const i32x4*)(qv + row * D + d), v2 = *(const i32x4*)(qv + row * D + d + 64);
  const s16x4 q1 = *(const s16x4*)(q + row * D + d), q2 = *(const s16x4*)(q + row * D + d + 64);
  const s16x4 c1 = *(const s16x4*)(kc + s * D + d), c2 = *(const s16x4*)(kc + s * D + d + 64);
  const s16x4 s1 = *(const s16x4*)(ks + s * D + d), s2 = *(const s16x4*)(ks + s * D + d + 64);
  const s16x4 qc1 = *(const s16x4*)(qc + s * D + d), qc2 = *(const s16x4*)(qc + s * D + d + 64);
  const s16x4 qs1 = *(const s16x4*)(qs + s * D + d), qs2 = *(const s16x4*)(qs + s * D + d + 64);
  const int g1 = d >> 5, g2 = g1 + 2;
  const float ks1 = __bfloat162float(ksc[row * 4 + g1]), ks2 = __bfloat162float(ksc[row * 4 + g2]);
  const float kz1 = __bfloat162float(kzp[row * 4 + g1]), kz2 = __bfloat162float(kzp[row * 4 + g2]);
  const float vs1 = __bfloat162float(vsc[row * 4 + g1]), vs2 = __bfloat162float(vsc[row * 4 + g2]);
  const float vz1 = __bfloat162float(vzp[row * 4 + g1]), vz2 = __bfloat162float(vzp[row * 4 + g2]);
  bf16 ko1[4], ko2[4], vo1[4], vo2[4], qo1[4], qo2[4];
#pragma unroll
  for (int j = 0; j < 4; ++j) {
    float a = rbf(rbf(rbf((float)k1[j]) - kz1) * ks1), b = rbf(rbf(rbf((float)k2[j]) - kz2) * ks2);
    ko1[j] = __float2bfloat16(rbf(a * bf2f(c1[j])) + rbf(-b * bf2f(s1[j])));
    ko2[j] = __float2bfloat16(rbf(b * bf2f(c2[j])) + rbf(a * bf2f(s2[j])));
    vo1[j] = __float2bfloat16(rbf(rbf((float)v1[j]) - vz1) * vs1);
    vo2[j] = __float2bfloat16(rbf(rbf((float)v2[j]) - vz2) * vs2);
    float qa = bf2f(q1[j]), qb = bf2f(q2[j]);
    qo1[j] = __float2bfloat16(rbf(qa * bf2f(qc1[j])) + rbf(-qb * bf2f(qs1[j])));
    qo2[j] = __float2bfloat16(rbf(qb * bf2f(qc2[j])) + rbf(qa * bf2f(qs2[j])));
  }
  *(s16x4*)(Ko + row * D + d) = *(s16x4*)ko1; *(s16x4*)(Ko + row * D + d + 64) = *(s16x4*)ko2;
  *(s16x4*)(Vo + row * D + d) = *(s16x4*)vo1; *(s16x4*)(Vo + row * D + d + 64) = *(s16x4*)vo2;
  *(s16x4*)(Qo + row * D + d) = *(s16x4*)qo1; *(s16x4*)(Qo + row * D + d + 64) = *(s16x4*)qo2;
}
extern "C" void kernel_launch(void* const* d_in, const int* in_sizes, int n_in, void* d_out, int out_size, void* d_ws, size_t ws_size, hipStream_t stream) {
  (void)hipFuncSetAttribute((const void*)attn_fwd_dense, hipFuncAttributeMaxDynamicSharedMemorySize, (int)SHM_ATTN);
  const size_t NE = (size_t)HQ * S * D;
  bf16* qb = (bf16*)d_ws; bf16* kb = qb + NE; bf16* vb = kb + NE;
  hipLaunchKernelGGL(prepass, dim3(HQ * S * 16 / 256), dim3(256), 0, stream, (const bf16*)d_in[0], (const int*)d_in[1], (const bf16*)d_in[2], (const bf16*)d_in[3],
     (const int*)d_in[4], (const bf16*)d_in[5], (const bf16*)d_in[6], (const bf16*)d_in[7], (const bf16*)d_in[8], (const bf16*)d_in[9], (const bf16*)d_in[10], qb, kb, vb);
  hipLaunchKernelGGL(attn_fwd_dense, dim3(256), dim3(64 * NW), SHM_ATTN, stream, qb, kb, vb, (bf16*)d_out);
}
```

```cpp
#include <hip/hip_runtime.h>
#include <hip/hip_bf16.h>

typedef unsigned short u16;
typedef __attribute__((ext_vector_type(8))) short bf16x8;
typedef __attribute__((ext_vector_type(4))) float f32x4;
typedef __attribute__((ext_vector_type(4))) unsigned u32x4;
typedef __attribute__((ext_vector_type(4))) int i32x4;
typedef __attribute__((ext_vector_type(2))) int i32x2;
typedef __attribute__((ext_vector_type(2))) float f32x2;
typedef __attribute__((ext_vector_type(2))) __bf16 bf16x2;

constexpr int H = 32, S = 2048, D = 128;
constexpr float QK_C = 0.12751743082f;
constexpr int LDS_BYTES = 0x24000 + 64;

struct Args {
  const u16* q; const int* qk; const u16* ksc; const u16* kzp; const int* qv; const u16* vsc; const u16* vzp;
  const u16* qc; const u16* qs; const u16* kc; const u16* ks; u16* out; char* ws;
};

__device__ __forceinline__ float bflo(unsigned u) { return __uint_as_float(u << 16); }
__device__ __forceinline__ float bfhi(unsigned u) { return __uint_as_float(u & 0xffff0000u); }
__device__ __forceinline__ float bf1(u16 u) { return __uint_as_float((unsigned)u << 16); }
__device__ __forceinline__ unsigned cvtpk(float lo, float hi) {
  f32x2 v = {lo, hi}; bf16x2 b = __builtin_convertvector(v, bf16x2); return __builtin_bit_cast(unsigned, b);
}

__device__ __forceinline__ void k_frags(const Args& a, long row, int key, int g, int half, u32x4& f0, u32x4& f1) {
  const int d0 = 16 * g + 8 * half;
  const i32x4 x0 = *(const i32x4*)(a.qk + row * D + d0), x1 = *(const i32x4*)(a.qk + row * D + d0 + 4);
  const i32x4 y0 = *(const i32x4*)(a.qk + row * D + d0 + 64), y1 = *(const i32x4*)(a.qk + row * D + d0 + 68);
  const u32x4 c1 = *(const u32x4*)(a.kc + key * D + d0), c2 = *(const u32x4*)(a.kc + key * D + d0 + 64);
  const u32x4 s1 = *(const u32x4*)(a.ks + key * D + d0), s2 = *(const u32x4*)(a.ks + key * D + d0 + 64);
  const int g1 = g >> 1, g2 = g1 + 2;
  const float sa = bf1(a.ksc[row * 4 + g1]) * QK_C, sb = bf1(a.ksc[row * 4 + g2]) * QK_C;
  const float za = bf1(a.kzp[row * 4 + g1]), zb = bf1(a.kzp[row * 4 + g2]);
  float r1[8], r2[8];
#pragma unroll
  for (int j = 0; j < 8; ++j) {
    const float k1 = ((float)(j < 4 ? x0[j & 3] : x1[j & 3]) - za) * sa, k2 = ((float)(j < 4 ? y0[j & 3] : y1[j & 3]) - zb) * sb;
    const float cc1 = (j & 1) ? bfhi(c1[j >> 1]) : bflo(c1[j >> 1]), cc2 = (j & 1) ? bfhi(c2[j >> 1]) : bflo(c2[j >> 1]);
    const float ss1 = (j & 1) ? bfhi(s1[j >> 1]) : bflo(s1[j >> 1]), ss2 = (j & 1) ? bfhi(s2[j >> 1]) : bflo(s2[j >> 1]);
    r1[j] = k1 * cc1 - k2 * ss1; r2[j] = k2 * cc2 + k1 * ss2;
  }
  f0 = u32x4{cvtpk(r1[0], r1[1]), cvtpk(r1[2], r1[3]), cvtpk(r2[0], r2[1]), cvtpk(r2[2], r2[3])};
  f1 = u32x4{cvtpk(r1[4], r1[5]), cvtpk(r1[6], r1[7]), cvtpk(r2[4], r2[5]), cvtpk(r2[6], r2[7])};
}
__device__ __forceinline__ void v_frags(const Args& a, long row0, int c, int w, u32x4& f0, u32x4& f1) {
  float e0[8], e1[8];
#pragma unroll
  for (int j = 0; j < 8; ++j) {
    const i32x2 x = *(const i32x2*)(a.qv + (row0 + j) * D + 32 * w + 2 * c);
    const float s = bf1(a.vsc[(row0 + j) * 4 + w]), z = bf1(a.vzp[(row0 + j) * 4 + w]);
    e0[j] = ((float)x[0] - z) * s; e1[j] = ((float)x[1] - z) * s;
  }
  f0 = u32x4{cvtpk(e0[0], e0[1]), cvtpk(e0[2], e0[3]), cvtpk(e0[4], e0[5]), cvtpk(e0[6], e0[7])};
  f1 = u32x4{cvtpk(e1[0], e1[1]), cvtpk(e1[2], e1[3]), cvtpk(e1[4], e1[5]), cvtpk(e1[6], e1[7])};
}

__device__ __forceinline__ void slow_path(const Args& a, int h, int row0) {
  const int lane = __builtin_amdgcn_mbcnt_hi(~0u, __builtin_amdgcn_mbcnt_lo(~0u, 0u)), c = lane & 15, g = lane >> 4;
#pragma unroll 1
  for (int qt = 0; qt < 4; ++qt) {
    const int qrow = row0 + 16 * qt + c;
    bf16x8 qf[4];
#pragma unroll
    for (int ds = 0; ds < 4; ++ds) {
      float r1[4], r2[4];
#pragma unroll
      for (int j = 0; j < 4; ++j) {
        const int d = 16 * g + 4 * ds + j;
        const float q1 = bf1(a.q[((long)h * S + qrow) * D + d]), q2 = bf1(a.q[((long)h * S + qrow) * D + d + 64]);
        r1[j] = q1 * bf1(a.qc[qrow * D + d]) - q2 * bf1(a.qs[qrow * D + d]);
        r2[j] = q2 * bf1(a.qc[qrow * D + d + 64]) + q1 * bf1(a.qs[qrow * D + d + 64]);
      }
      u32x4 t = {cvtpk(r1[0], r1[1]), cvtpk(r1[2], r1[3]), cvtpk(r2[0], r2[1]), cvtpk(r2[2], r2[3])};
      qf[ds] = __builtin_bit_cast(bf16x8, t);
    }
    float m = -__builtin_inff(), l = 0.f;
    f32x4 o[8];
#pragma unroll
    for (int i = 0; i < 8; ++i) o[i] = f32x4{0.f, 0.f, 0.f, 0.f};
#pragma unroll 1
    for (int blk = 0; blk < S / 32; ++blk) {
      f32x4 s[2];
#pragma unroll
      for (int kt = 0; kt < 2; ++kt) {
        const int key = 32 * blk + 8 * (c >> 2) + 4 * kt + (c & 3);
        s[kt] = f32x4{0.f, 0.f, 0.f, 0.f};
#pragma unroll
        for (int hf = 0; hf < 2; ++hf) {
          u32x4 f0, f1; k_frags(a, (long)h * S + key, key, g, hf, f0, f1);
          s[kt] = __builtin_amdgcn_mfma_f32_16x16x32_bf16(__builtin_bit_cast(bf16x8, f0), qf[2 * hf], s[kt], 0, 0, 0);
          s[kt] = __builtin_amdgcn_mfma_f32_16x16x32_bf16(__builtin_bit_cast(bf16x8, f1), qf[2 * hf + 1], s[kt], 0, 0, 0);
        }
      }
      float mx = fmaxf(fmaxf(fmaxf(s[0][0], s[0][1]), fmaxf(s[0][2], s[0][3])), fmaxf(fmaxf(s[1][0], s[1][1]), fmaxf(s[1][2], s[1][3])));
      mx = fmaxf(mx, __shfl_xor(mx, 16)); mx = fmaxf(mx, __shfl_xor(mx, 32));
      const float mn = fmaxf(m, mx), al = __builtin_amdgcn_exp2f(m - mn);
      m = mn;
      float p[8], ps = 0.f;
#pragma unroll
      for (int i = 0; i < 8; ++i) { p[i] = __builtin_amdgcn_exp2f(s[i >> 2][i & 3] - mn); ps += p[i]; }
      l = l * al + ps;
      u32x4 pk = {cvtpk(p[0], p[1]), cvtpk(p[2], p[3]), cvtpk(p[4], p[5]), cvtpk(p[6], p[7])};
#pragma unroll
      for (int w = 0; w < 4; ++w) {
        u32x4 f0, f1; v_frags(a, (long)h * S + 32 * blk + 8 * g, c, w, f0, f1);
        o[2 * w] = __builtin_amdgcn_mfma_f32_16x16x32_bf16(__builtin_bit_cast(bf16x8, f0), __builtin_bit_cast(bf16x8, pk), o[2 * w] * al, 0, 0, 0);
        o[2 * w + 1] = __builtin_amdgcn_mfma_f32_16x16x32_bf16(__builtin_bit_cast(bf16x8, f1), __builtin_bit_cast(bf16x8, pk), o[2 * w + 1] * al, 0, 0, 0);
      }
    }
    l += __shfl_xor(l, 16); l += __shfl_xor(l, 32);
    const float r = 1.f / l;
#pragma unroll
    for (int w = 0; w < 4; ++w) {
      u32x4 t = {cvtpk(o[2 * w][0] * r, o[2 * w + 1][0] * r), cvtpk(o[2 * w][1] * r, o[2 * w + 1][1] * r),
                 cvtpk(o[2 * w][2] * r, o[2 * w + 1][2] * r), cvtpk(o[2 * w][3] * r, o[2 * w + 1][3] * r)};
      *(u32x4*)(a.out + ((long)h * S + qrow) * D + 32 * w + 8 * g) = t;
    }
  }
}

__global__ __launch_bounds__(256, 1) void attn(Args a) {
  extern __shared__ __attribute__((aligned(16))) char lds[];
  const unsigned wid = __builtin_amdgcn_readfirstlane(threadIdx.x >> 6);
  const unsigned bid = blockIdx.x, idx = bid >> 3;
  const unsigned h = 4 * (bid & 7) + (idx >> 3), j = idx & 7;
  const unsigned ldsb = (unsigned)(size_t)lds;
  unsigned fail;
  asm volatile(
      "s_load_dwordx16 s[36:51], %1, 0\n\t"
      "s_load_dwordx8 s[52:59], %1, 64\n\t"
      "s_load_dwordx2 s[60:61], %1, 96\n\t"
      "v_mbcnt_lo_u32_b32 v240, -1, 0\n\t"
      "v_mbcnt_hi_u32_b32 v240, -1, v240\n\t"
      "v_lshlrev_b32 v152, 4, v240\n\t"
      "s_lshl_b32 s87, %5, 2\n\t"
      "s_add_u32 s87, s87, %2\n\t"
      "s_getpc_b64 s[72:73]\n\t"
      "s_mov_b32 s74, 25600\n\t"
      "s_mov_b32 s75, 0x20000\n\t"
      "s_lshl_b32 s88, s87, 10\n\t"
      "buffer_load_dwordx4 v[248:251], v152, s[72:75], s88 offen\n\t"
      "v_lshrrev_b32 v242, 4, v240\n\t"
      "v_lshrrev_b32 v243, 3, v240\n\t"
      "v_and_b32 v244, 7, v240\n\t"
      "v_lshlrev_b32 v153, 9, v243\n\t"
      "v_lshl_add_u32 v153, v244, 5, v153\n\t"
      "v_lshrrev_b32 v154, 1, v153\n\t"
      "v_lshlrev_b32 v155, 3, v243\n\t"
      "v_lshlrev_b32 v159, 3, v240\n\t"
      "v_lshlrev_b32 v160, 3, v244\n\t"
      "v_lshl_add_u32 v160, v242, 1, v160\n\t"
      "v_and_b32 v162, 0xf8, v159\n\t"
      "v_lshrrev_b32 v245, 2, v162\n\t"
      "v_and_b32 v245, 0x38, v245\n\t"
      "s_lshl_b32 s90, s87, 12\n\t"
      "s_lshl_b32 s91, s87, 11\n\t"
      "s_lshl_b32 s92, s87, 6\n\t"
      "s_lshl_b32 s24, s87, 14\n\t"
      "s_lshl_b32 s25, %4, 19\n\t"
      "s_add_u32 s25, s25, s24\n\t"
      "s_lshl_b32 s26, %4, 20\n\t"
      "s_lshl_b32 s27, %4, 14\n\t"
      "s_mul_i32 s30, %4, 1064960\n\t"
      "s_mov_b32 s94, 0\n\t"
      "s_and_b32 s97, %4, 3\n\t"
      "s_lshl_b32 s97, s97, 12\n\t"
      "s_mov_b32 s74, 16384\n\t"
      "s_mov_b32 s75, 0x20000\n\t"
      "s_mov_b32 s78, 16384\n\t"
      "s_mov_b32 s79, 0x20000\n\t"
      "s_mov_b32 s82, 16384\n\t"
      "s_mov_b32 s83, 0x20000\n\t"
      "s_mov_b32 s70, 16384\n\t"
      "s_mov_b32 s71, 0x20000\n\t"
      "s_mov_b32 s66, 16384\n\t"
      "s_mov_b32 s67, 0x20000\n\t"
      "s_mov_b32 s62, 1048576\n\t"
      "s_mov_b32 s63, 0x20000\n\t"
      "s_waitcnt lgkmcnt(0)\n\t"
      "s_add_u32 s72, s36, s25\n\t"
      "s_addc_u32 s73, s37, 0\n\t"
      "s_add_u32 s76, s50, s24\n\t"
      "s_addc_u32 s77, s51, 0\n\t"
      "s_add_u32 s80, s52, s24\n\t"
      "s_addc_u32 s81, s53, 0\n\t"
      "s_add_u32 s98, s58, s25\n\t"
      "s_addc_u32 s99, s59, 0\n\t"
      "s_add_u32 s68, s48, s27\n\t"
      "s_addc_u32 s69, s49, 0\n\t"
      "s_add_u32 s64, s46, s27\n\t"
      "s_addc_u32 s65, s47, 0\n\t"
      "s_add_u32 s36, s60, s30\n\t"
      "s_addc_u32 s37, s61, 0\n\t"
      "s_add_u32 s60, s44, s26\n\t"
      "s_addc_u32 s61, s45, 0\n\t"
      "s_mov_b64 s[48:49], s[56:57]\n\t"
      "s_mov_b64 s[44:45], s[54:55]\n\t"
      "s_add_u32 s56, s42, s27\n\t"
      "s_addc_u32 s57, s43, 0\n\t"
      "s_add_u32 s52, s40, s27\n\t"
      "s_addc_u32 s53, s41, 0\n\t"
      "s_add_u32 s40, s38, s26\n\t"
      "s_addc_u32 s41, s39, 0\n\t"
      "s_mov_b32 s38, 1064960\n\t"
      "s_mov_b32 s39, 0x20000\n\t"
      "s_mov_b32 s50, 524288\n\t"
      "s_mov_b32 s51, 0x20000\n\t"
      "s_mov_b32 s46, 524288\n\t"
      "s_mov_b32 s47, 0x20000\n\t"
      "s_mov_b32 s58, 16384\n\t"
      "s_mov_b32 s59, 0x20000\n\t"
      "s_mov_b32 s54, 16384\n\t"
      "s_mov_b32 s55, 0x20000\n\t"
      "s_mov_b32 s42, 1048576\n\t"
      "s_mov_b32 s43, 0x20000\n\t"
      "buffer_load_dwordx4 v[170:173], v153, s[40:43], s90 offen offset:0\n\t"
      "buffer_load_dwordx4 v[174:177], v153, s[40:43], s90 offen offset:16\n\t"
      "buffer_load_dwordx4 v[178:181], v153, s[40:43], s90 offen offset:256\n\t"
      "buffer_load_dwordx4 v[182:185], v153, s[40:43], s90 offen offset:272\n\t"
      "buffer_load_dwordx2 v[206:207], v159, s[60:63], s90 offen offset:0 nt\n\t"
      "buffer_load_dwordx2 v[208:209], v159, s[60:63], s90 offen offset:512 nt\n\t"
      "buffer_load_dwordx2 v[210:211], v159, s[60:63], s90 offen offset:1024 nt\n\t"
      "buffer_load_dwordx2 v[212:213], v159, s[60:63], s90 offen offset:1536 nt\n\t"
      "buffer_load_dwordx2 v[214:215], v159, s[60:63], s90 offen offset:2048 nt\n\t"
      "buffer_load_dwordx2 v[216:217], v159, s[60:63], s90 offen offset:2560 nt\n\t"
      "buffer_load_dwordx2 v[218:219], v159, s[60:63], s90 offen offset:3072 nt\n\t"
      "buffer_load_dwordx2 v[220:221], v159, s[60:63], s90 offen offset:3584 nt\n\t"
      "buffer_load_dwordx2 v[202:203], v155, s[52:55], s92 offen\n\t"
      "buffer_load_dwordx2 v[204:205], v155, s[56:59], s92 offen\n\t"
      "buffer_load_ushort v222, v160, s[64:67], s92 offen\n\t"
      "buffer_load_ushort v223, v160, s[68:71], s92 offen\n\t"
      "buffer_load_dwordx4 v[186:189], v154, s[44:47], s91 offen offset:0\n\t"
      "buffer_load_dwordx4 v[190:193], v154, s[44:47], s91 offen offset:128\n\t"
      "buffer_load_dwordx4 v[194:197], v154, s[48:51], s91 offen offset:0\n\t"
      "buffer_load_dwordx4 v[198:201], v154, s[48:51], s91 offen offset:128\n\t"
      "s_add_u32 s90, s90, 131072\n\t"
      "s_add_u32 s91, s91, 65536\n\t"
      "s_add_u32 s92, s92, 2048\n\t"
      "buffer_load_dwordx2 v[164:165], v245, s[36:39], s94 offen sc1\n\t"
      "s_add_u32 s88, s97, 0\n\t"
      "s_and_b32 s88, s88, 0x3fff\n\t"
      "buffer_load_dwordx4 v[0:3], v154, s[72:75], s88 offen offset:0 nt\n\t"
      "buffer_load_dwordx4 v[4:7], v154, s[72:75], s88 offen offset:128 nt\n\t"
      "buffer_load_dwordx4 v[8:11], v154, s[76:79], s88 offen offset:0\n\t"
      "buffer_load_dwordx4 v[12:15], v154, s[76:79], s88 offen offset:128\n\t"
      "buffer_load_dwordx4 v[16:19], v154, s[80:83], s88 offen offset:0\n\t"
      "buffer_load_dwordx4 v[20:23], v154, s[80:83], s88 offen offset:128\n\t"
      "v_mov_b32 v224, 0\n\t"
      "v_mov_b32 v225, 0\n\t"
      "v_mov_b32 v226, 0\n\t"
      "v_mov_b32 v227, 0\n\t"
      "s_mov_b32 s89, 0xffff0000\n\t"
      "s_mov_b32 s95, 0x3e0293ee\n\t"
      "s_mov_b32 s96, 20000\n\t"
      "s_mov_b32 s31, 0x05040100\n\t"
      "s_mov_b32 s84, 0x07060302\n\t"
      "v_and_b32 v241, 15, v240\n\t"
      "v_lshrrev_b32 v245, 2, v244\n\t"
      "v_mul_u32_u24 v245, 0x202, v245\n\t"
      "v_lshlrev_b32 v245, 16, v245\n\t"
      "v_add_u32 v156, 0x01000c0c, v245\n\t"
      "s_lshl_b32 s88, %2, 2\n\t"
      "v_and_b32 v246, 3, v243\n\t"
      "v_add_u32 v246, s88, v246\n\t"
      "v_xor_b32 v245, v244, v246\n\t"
      "v_lshlrev_b32 v245, 4, v245\n\t"
      "v_lshl_add_u32 v245, v246, 8, v245\n\t"
      "v_lshrrev_b32 v157, 2, v243\n\t"
      "v_lshl_add_u32 v157, v157, 12, v245\n\t"
      "v_xor_b32 v158, 0x80, v157\n\t"
      "v_and_b32 v245, 3, v240\n\t"
      "v_xor_b32 v245, v245, v242\n\t"
      "v_lshlrev_b32 v245, 4, v245\n\t"
      "v_lshrrev_b32 v161, 2, v240\n\t"
      "v_lshl_add_u32 v161, v161, 6, v245\n\t"
      "s_lshl_b32 s88, %2, 11\n\t"
      "s_add_u32 s88, s88, 16384\n\t"
      "v_add_u32 v161, s88, v161\n\t"
      "s_lshr_b32 s93, %5, 1\n\t"
      "s_lshl_b32 s93, s93, 15\n\t"
      "s_and_b32 s88, %5, 1\n\t"
      "s_lshl_b32 s88, s88, 13\n\t"
      "s_add_u32 s93, s93, s88\n\t"
      "s_add_u32 s93, s93, 16384\n\t"
      "v_min_u32 v245, 1, v240\n\t"
      "v_lshlrev_b32 v245, 24, v245\n\t"
      "s_lshl_b32 s88, s87, 3\n\t"
      "v_add_u32 v163, s88, v245\n\t"
      "v_xor_b32 v245, v242, v241\n\t"
      "v_lshlrev_b32 v245, 4, v245\n\t"
      "v_lshl_add_u32 v245, v241, 8, v245\n\t"
      "v_xor_b32 v136, 0, v245\n\t"
      "v_add_u32 v136, %3, v136\n\t"
      "v_xor_b32 v137, 64, v245\n\t"
      "v_add_u32 v137, %3, v137\n\t"
      "v_xor_b32 v138, 128, v245\n\t"
      "v_add_u32 v138, %3, v138\n\t"
      "v_xor_b32 v139, 192, v245\n\t"
      "v_add_u32 v139, %3, v139\n\t"
      "v_lshrrev_b32 v245, 2, v241\n\t"
      "v_lshlrev_b32 v245, 4, v245\n\t"
      "v_lshl_add_u32 v245, v241, 6, v245\n\t"
      "v_lshl_add_u32 v245, v242, 11, v245\n\t"
      "v_xor_b32 v144, 0, v245\n\t"
      "v_add_u32 v144, %3, v144\n\t"
      "v_xor_b32 v145, 16, v245\n\t"
      "v_add_u32 v145, %3, v145\n\t"
      "v_xor_b32 v146, 32, v245\n\t"
      "v_add_u32 v146, %3, v146\n\t"
      "v_xor_b32 v147, 48, v245\n\t"
      "v_add_u32 v147, %3, v147\n\t"
      "s_lshl_b32 s88, %2, 12\n\t"
      "s_add_u32 s88, s88, 131072\n\t"
      "v_xor_b32 v245, v244, v243\n\t"
      "v_lshlrev_b32 v245, 4, v245\n\t"
      "v_lshl_add_u32 v245, v243, 8, v245\n\t"
      "v_add_u32 v245, s88, v245\n\t"
      "v_add_u32 v252, %3, v245\n\t"
      "v_xor_b32 v253, 0x80, v245\n\t"
      "v_add_u32 v253, %3, v253\n\t"
      "v_add_u32 v148, s88, v136\n\t"
      "v_add_u32 v149, s88, v137\n\t"
      "v_add_u32 v150, s88, v138\n\t"
      "v_add_u32 v151, s88, v139\n\t"
      "v_mov_b32 v245, %3\n\t"
      "v_add_u32 v245, 147456, v245\n\t"
      "ds_write_b32 v245, v224\n\t"
      "s_lshl_b32 s88, %2, 13\n\t"
      "s_add_u32 s85, s88, %3\n\t"
      "s_add_u32 s88, s88, 16384\n\t"
      "s_and_b32 s86, %5, 3\n\t"
      "s_add_u32 s24, s86, 0\n\t"
      "s_and_b32 s24, s24, 3\n\t"
      "s_lshl_b32 s24, s24, 15\n\t"
      "s_add_u32 s24, s24, s88\n\t"
      "s_add_u32 s25, s86, 1\n\t"
      "s_and_b32 s25, s25, 3\n\t"
      "s_lshl_b32 s25, s25, 15\n\t"
      "s_add_u32 s25, s25, s88\n\t"
      "s_add_u32 s26, s86, 2\n\t"
      "s_and_b32 s26, s26, 3\n\t"
      "s_lshl_b32 s26, s26, 15\n\t"
      "s_add_u32 s26, s26, s88\n\t"
      "s_add_u32 s27, s86, 3\n\t"
      "s_and_b32 s27, s27, 3\n\t"
      "s_lshl_b32 s27, s27, 15\n\t"
      "s_add_u32 s27, s27, s88\n\t"
      "s_waitcnt lgkmcnt(0)\n\t"
      "s_barrier\n\t"
      "s_waitcnt vmcnt(6)\n\t"
      "v_perm_b32 v224, v202, v202, v156\n\t"
      "v_perm_b32 v226, v203, v203, v156\n\t"
      "v_perm_b32 v228, v204, v204, v156\n\t"
      "v_perm_b32 v230, v205, v205, v156\n\t"
      "v_mul_f32 v224, s95, v224\n\t"
      "v_mul_f32 v226, s95, v226\n\t"
      "v_mul_f32_e64 v228, -v228, v224\n\t"
      "v_mul_f32_e64 v230, -v230, v226\n\t"
      "v_cvt_f32_i32 v170, v170\n\t"
      "v_cvt_f32_i32 v171, v171\n\t"
      "v_cvt_f32_i32 v172, v172\n\t"
      "v_cvt_f32_i32 v173, v173\n\t"
      "v_cvt_f32_i32 v174, v174\n\t"
      "v_cvt_f32_i32 v175, v175\n\t"
      "v_cvt_f32_i32 v176, v176\n\t"
      "v_cvt_f32_i32 v177, v177\n\t"
      "v_cvt_f32_i32 v178, v178\n\t"
      "v_cvt_f32_i32 v179, v179\n\t"
      "v_cvt_f32_i32 v180, v180\n\t"
      "v_cvt_f32_i32 v181, v181\n\t"
      "v_cvt_f32_i32 v182, v182\n\t"
      "v_cvt_f32_i32 v183, v183\n\t"
      "v_cvt_f32_i32 v184, v184\n\t"
      "v_cvt_f32_i32 v185, v185\n\t"
      "v_pk_fma_f32 v[170:171], v[170:171], v[224:225], v[228:229] op_sel_hi:[1,0,0]\n\t"
      "v_pk_fma_f32 v[172:173], v[172:173], v[224:225], v[228:229] op_sel_hi:[1,0,0]\n\t"
      "v_pk_fma_f32 v[174:175], v[174:175], v[224:225], v[228:229] op_sel_hi:[1,0,0]\n\t"
      "v_pk_fma_f32 v[176:177], v[176:177], v[224:225], v[228:229] op_sel_hi:[1,0,0]\n\t"
      "v_pk_fma_f32 v[178:179], v[178:179], v[226:227], v[230:231] op_sel_hi:[1,0,0]\n\t"
      "v_pk_fma_f32 v[180:181], v[180:181], v[226:227], v[230:231] op_sel_hi:[1,0,0]\n\t"
      "v_pk_fma_f32 v[182:183], v[182:183], v[226:227], v[230:231] op_sel_hi:[1,0,0]\n\t"
      "v_pk_fma_f32 v[184:185], v[184:185], v[226:227], v[230:231] op_sel_hi:[1,0,0]\n\t"
      "v_lshlrev_b32 v232, 16, v186\n\t"
      "v_and_b32 v233, s89, v186\n\t"
      "v_lshlrev_b32 v234, 16, v190\n\t"
      "v_and_b32 v235, s89, v190\n\t"
      "v_lshlrev_b32 v236, 16, v194\n\t"
      "v_and_b32 v237, s89, v194\n\t"
      "v_lshlrev_b32 v238, 16, v198\n\t"
      "v_and_b32 v239, s89, v198\n\t"
      "v_pk_mul_f32 v[232:233], v[170:171], v[232:233]\n\t"
      "v_pk_mul_f32 v[234:235], v[178:179], v[234:235]\n\t"
      "v_pk_fma_f32 v[232:233], v[178:179], v[236:237], v[232:233] neg_lo:[1,0,0] neg_hi:[1,0,0]\n\t"
      "v_pk_fma_f32 v[234:235], v[170:171], v[238:239], v[234:235]\n\t"
      "v_cvt_pk_bf16_f32 v240, v232, v233\n\t"
      "v_cvt_pk_bf16_f32 v244, v234, v235\n\t"
      "v_lshlrev_b32 v232, 16, v187\n\t"
      "v_and_b32 v233, s89, v187\n\t"
      "v_lshlrev_b32 v234, 16, v191\n\t"
      "v_and_b32 v235, s89, v191\n\t"
      "v_lshlrev_b32 v236, 16, v195\n\t"
      "v_and_b32 v237, s89, v195\n\t"
      "v_lshlrev_b32 v238, 16, v199\n\t"
      "v_and_b32 v239, s89, v199\n\t"
      "v_pk_mul_f32 v[232:233], v[172:173], v[232:233]\n\t"
      "v_pk_mul_f32 v[234:235], v[180:181], v[234:235]\n\t"
      "v_pk_fma_f32 v[232:233], v[180:181], v[236:237], v[232:233] neg_lo:[1,0,0] neg_hi:[1,0,0]\n\t"
      "v_pk_fma_f32 v[234:235], v[172:173], v[238:239], v[234:235]\n\t"
      "v_cvt_pk_bf16_f32 v241, v232, v233\n\t"
      "v_cvt_pk_bf16_f32 v245, v234, v235\n\t"
      "v_lshlrev_b32 v232, 16, v188\n\t"
      "v_and_b32 v233, s89, v188\n\t"
      "v_lshlrev_b32 v234, 16, v192\n\t"
      "v_and_b32 v235, s89, v192\n\t"
      "v_lshlrev_b32 v236, 16, v196\n\t"
      "v_and_b32 v237, s89, v196\n\t"
      "v_lshlrev_b32 v238, 16, v200\n\t"
      "v_and_b32 v239, s89, v200\n\t"
      "v_pk_mul_f32 v[232:233], v[174:175], v[232:233]\n\t"
      "v_pk_mul_f32 v[234:235], v[182:183], v[234:235]\n\t"
      "v_pk_fma_f32 v[232:233], v[182:183], v[236:237], v[232:233] neg_lo:[1,0,0] neg_hi:[1,0,0]\n\t"
      "v_pk_fma_f32 v[234:235], v[174:175], v[238:239], v[234:235]\n\t"
      "v_cvt_pk_bf16_f32 v242, v232, v233\n\t"
      "v_cvt_pk_bf16_f32 v246, v234, v235\n\t"
      "v_lshlrev_b32 v232, 16, v189\n\t"
      "v_and_b32 v233, s89, v189\n\t"
      "v_lshlrev_b32 v234, 16, v193\n\t"
      "v_and_b32 v235, s89, v193\n\t"
      "v_lshlrev_b32 v236, 16, v197\n\t"
      "v_and_b32 v237, s89, v197\n\t"
      "v_lshlrev_b32 v238, 16, v201\n\t"
      "v_and_b32 v239, s89, v201\n\t"
      "v_pk_mul_f32 v[232:233], v[176:177], v[232:233]\n\t"
      "v_pk_mul_f32 v[234:235], v[184:185], v[234:235]\n\t"
      "v_pk_fma_f32 v[232:233], v[184:185], v[236:237], v[232:233] neg_lo:[1,0,0] neg_hi:[1,0,0]\n\t"
      "v_pk_fma_f32 v[234:235], v[176:177], v[238:239], v[234:235]\n\t"
      "v_cvt_pk_bf16_f32 v243, v232, v233\n\t"
      "v_cvt_pk_bf16_f32 v247, v234, v235\n\t"
      "buffer_store_dwordx4 v[240:243], v157, s[36:39], s93 offen sc1\n\t"
      "buffer_store_dwordx4 v[244:247], v158, s[36:39], s93 offen sc1\n\t"
      "v_lshlrev_b32 v222, 16, v222\n\t"
      "v_lshlrev_b32 v223, 16, v223\n\t"
      "v_mul_f32_e64 v223, -v223, v222\n\t"
      "v_cvt_f32_i32 v206, v206\n\t"
      "v_cvt_f32_i32 v207, v207\n\t"
      "v_cvt_f32_i32 v208, v208\n\t"
      "v_cvt_f32_i32 v209, v209\n\t"
      "v_cvt_f32_i32 v210, v210\n\t"
      "v_cvt_f32_i32 v211, v211\n\t"
      "v_cvt_f32_i32 v212, v212\n\t"
      "v_cvt_f32_i32 v213, v213\n\t"
      "v_cvt_f32_i32 v214, v214\n\t"
      "v_cvt_f32_i32 v215, v215\n\t"
      "v_cvt_f32_i32 v216, v216\n\t"
      "v_cvt_f32_i32 v217, v217\n\t"
      "v_cvt_f32_i32 v218, v218\n\t"
      "v_cvt_f32_i32 v219, v219\n\t"
      "v_cvt_f32_i32 v220, v220\n\t"
      "v_cvt_f32_i32 v221, v221\n\t"
      "v_mov_b32_dpp v224, v222 row_newbcast:0 row_mask:0xf bank_mask:0xf\n\t"
      "v_mov_b32_dpp v226, v223 row_newbcast:0 row_mask:0xf bank_mask:0xf\n\t"
      "v_pk_fma_f32 v[206:207], v[206:207], v[224:225], v[226:227] op_sel_hi:[1,0,0]\n\t"
      "v_mov_b32_dpp v228, v222 row_newbcast:1 row_mask:0xf bank_mask:0xf\n\t"
      "v_mov_b32_dpp v230, v223 row_newbcast:1 row_mask:0xf bank_mask:0xf\n\t"
      "v_pk_fma_f32 v[208:209], v[208:209], v[228:229], v[230:231] op_sel_hi:[1,0,0]\n\t"
      "v_mov_b32_dpp v224, v222 row_newbcast:2 row_mask:0xf bank_mask:0xf\n\t"
      "v_mov_b32_dpp v226, v223 row_newbcast:2 row_mask:0xf bank_mask:0xf\n\t"
      "v_pk_fma_f32 v[210:211], v[210:211], v[224:225], v[226:227] op_sel_hi:[1,0,0]\n\t"
      "v_mov_b32_dpp v228, v222 row_newbcast:3 row_mask:0xf bank_mask:0xf\n\t"
      "v_mov_b32_dpp v230, v223 row_newbcast:3 row_mask:0xf bank_mask:0xf\n\t"
      "v_pk_fma_f32 v[212:213], v[212:213], v[228:229], v[230:231] op_sel_hi:[1,0,0]\n\t"
      "v_mov_b32_dpp v224, v222 row_newbcast:4 row_mask:0xf bank_mask:0xf\n\t"
      "v_mov_b32_dpp v226, v223 row_newbcast:4 row_mask:0xf bank_mask:0xf\n\t"
      "v_pk_fma_f32 v[214:215], v[214:215], v[224:225], v[226:227] op_sel_hi:[1,0,0]\n\t"
      "v_mov_b32_dpp v228, v222 row_newbcast:5 row_mask:0xf bank_mask:0xf\n\t"
      "v_mov_b32_dpp v230, v223 row_newbcast:5 row_mask:0xf bank_mask:0xf\n\t"
      "v_pk_fma_f32 v[216:217], v[216:217], v[228:229], v[230:231] op_sel_hi:[1,0,0]\n\t"
      "v_mov_b32_dpp v224, v222 row_newbcast:6 row_mask:0xf bank_mask:0xf\n\t"
      "v_mov_b32_dpp v226, v223 row_newbcast:6 row_mask:0xf bank_mask:0xf\n\t"
      "v_pk_fma_f32 v[218:219], v[218:219], v[224:225], v[226:227] op_sel_hi:[1,0,0]\n\t"
      "v_mov_b32_dpp v228, v222 row_newbcast:7 row_mask:0xf bank_mask:0xf\n\t"
      "v_mov_b32_dpp v230, v223 row_newbcast:7 row_mask:0xf bank_mask:0xf\n\t"
      "v_pk_fma_f32 v[220:221], v[220:221], v[228:229], v[230:231] op_sel_hi:[1,0,0]\n\t"
      "v_cvt_pk_bf16_f32 v232, v206, v208\n\t"
      "v_cvt_pk_bf16_f32 v233, v210, v212\n\t"
      "v_cvt_pk_bf16_f32 v234, v214, v216\n\t"
      "v_cvt_pk_bf16_f32 v235, v218, v220\n\t"
      "buffer_store_dwordx4 v[232:235], v161, s[36:39], s93 offen offset:0 sc1\n\t"
      "v_cvt_pk_bf16_f32 v236, v207, v209\n\t"
      "v_cvt_pk_bf16_f32 v237, v211, v213\n\t"
      "v_cvt_pk_bf16_f32 v238, v215, v217\n\t"
      "v_cvt_pk_bf16_f32 v239, v219, v221\n\t"
      "buffer_store_dwordx4 v[236:239], v161, s[36:39], s93 offen offset:1024 sc1\n\t"
      "s_add_u32 s93, s93, 131072\n\t"
      "s_mov_b32 s88, 0x9e3779b1\n\t"
      "v_mul_lo_u32 v164, v164, s88\n\t"
      "v_add_u32 v164, 0x7f4a7c15, v164\n\t"
      "s_mov_b32 s88, 0x85ebca77\n\t"
      "v_mul_lo_u32 v165, v165, s88\n\t"
      "v_add_u32 v165, 0xc2b2ae3d, v165\n\t"
      "s_add_u32 s30, s94, 256\n\t"
      "v_readlane_b32 s88, v165, s87\n\t"
      "v_readlane_b32 s87, v164, s87\n\t"
      "s_add_u32 s94, s94, 512\n\t"
      "s_mov_b32 s86, 8\n\t"
      "v_mov_b32 v168, s87\n\t"
      "v_mov_b32 v169, s88\n\t"
      "s_add_u32 s88, s97, 2048\n\t"
      "s_and_b32 s88, s88, 0x3fff\n\t"
      "buffer_load_dwordx4 v[24:27], v154, s[72:75], s88 offen offset:0 nt\n\t"
      "buffer_load_dwordx4 v[28:31], v154, s[72:75], s88 offen offset:128 nt\n\t"
      "buffer_load_dwordx4 v[32:35], v154, s[76:79], s88 offen offset:0\n\t"
      "buffer_load_dwordx4 v[36:39], v154, s[76:79], s88 offen offset:128\n\t"
      "buffer_load_dwordx4 v[40:43], v154, s[80:83], s88 offen offset:0\n\t"
      "buffer_load_dwordx4 v[44:47], v154, s[80:83], s88 offen offset:128\n\t"
      "s_add_u32 s88, s97, 4096\n\t"
      "s_and_b32 s88, s88, 0x3fff\n\t"
      "buffer_load_dwordx4 v[48:51], v154, s[72:75], s88 offen offset:0 nt\n\t"
      "buffer_load_dwordx4 v[52:55], v154, s[72:75], s88 offen offset:128 nt\n\t"
      "buffer_load_dwordx4 v[56:59], v154, s[76:79], s88 offen offset:0\n\t"
      "buffer_load_dwordx4 v[60:63], v154, s[76:79], s88 offen offset:128\n\t"
      "buffer_load_dwordx4 v[64:67], v154, s[80:83], s88 offen offset:0\n\t"
      "buffer_load_dwordx4 v[68:71], v154, s[80:83], s88 offen offset:128\n\t"
      "s_waitcnt vmcnt(12)\n\t"
      "buffer_store_dwordx2 v[168:169], v163, s[36:39], s30 offen sc1\n\t"
      "s_add_u32 s88, s97, 6144\n\t"
      "s_and_b32 s88, s88, 0x3fff\n\t"
      "buffer_load_dwordx4 v[72:75], v154, s[72:75], s88 offen offset:0 nt\n\t"
      "buffer_load_dwordx4 v[76:79], v154, s[72:75], s88 offen offset:128 nt\n\t"
      "buffer_load_dwordx4 v[80:83], v154, s[76:79], s88 offen offset:0\n\t"
      "buffer_load_dwordx4 v[84:87], v154, s[76:79], s88 offen offset:128\n\t"
      "buffer_load_dwordx4 v[88:91], v154, s[80:83], s88 offen offset:0\n\t"
      "buffer_load_dwordx4 v[92:95], v154, s[80:83], s88 offen offset:128\n\t"
      "s_waitcnt vmcnt(23)\n\t"
      "v_perm_b32 v230, v4, v0, s31\n\t"
      "v_perm_b32 v231, v16, v8, s31\n\t"
      "v_perm_b32 v232, v12, v20, s31\n\t"
      "v_perm_b32 v233, v4, v0, s84\n\t"
      "v_perm_b32 v234, v16, v8, s84\n\t"
      "v_perm_b32 v235, v12, v20, s84\n\t"
      "v_dot2_f32_bf16 v236, v230, v231, 0 neg_hi:[1,0,0]\n\t"
      "v_dot2_f32_bf16 v238, v230, v232, 0\n\t"
      "v_dot2_f32_bf16 v237, v233, v234, 0 neg_hi:[1,0,0]\n\t"
      "v_dot2_f32_bf16 v239, v233, v235, 0\n\t"
      "v_perm_b32 v230, v5, v1, s31\n\t"
      "v_perm_b32 v231, v17, v9, s31\n\t"
      "v_perm_b32 v232, v13, v21, s31\n\t"
      "v_cvt_pk_bf16_f32 v240, v236, v237\n\t"
      "v_cvt_pk_bf16_f32 v244, v238, v239\n\t"
      "v_perm_b32 v233, v5, v1, s84\n\t"
      "v_perm_b32 v234, v17, v9, s84\n\t"
      "v_perm_b32 v235, v13, v21, s84\n\t"
      "v_dot2_f32_bf16 v236, v230, v231, 0 neg_hi:[1,0,0]\n\t"
      "v_dot2_f32_bf16 v238, v230, v232, 0\n\t"
      "v_dot2_f32_bf16 v237, v233, v234, 0 neg_hi:[1,0,0]\n\t"
      "v_dot2_f32_bf16 v239, v233, v235, 0\n\t"
      "v_perm_b32 v230, v6, v2, s31\n\t"
      "v_perm_b32 v231, v18, v10, s31\n\t"
      "v_perm_b32 v232, v14, v22, s31\n\t"
      "v_cvt_pk_bf16_f32 v241, v236, v237\n\t"
      "v_cvt_pk_bf16_f32 v245, v238, v239\n\t"
      "v_perm_b32 v233, v6, v2, s84\n\t"
      "v_perm_b32 v234, v18, v10, s84\n\t"
      "v_perm_b32 v235, v14, v22, s84\n\t"
      "v_dot2_f32_bf16 v236, v230, v231, 0 neg_hi:[1,0,0]\n\t"
      "v_dot2_f32_bf16 v238, v230, v232, 0\n\t"
      "v_dot2_f32_bf16 v237, v233, v234, 0 neg_hi:[1,0,0]\n\t"
      "v_dot2_f32_bf16 v239, v233, v235, 0\n\t"
      "v_perm_b32 v230, v7, v3, s31\n\t"
      "v_perm_b32 v231, v19, v11, s31\n\t"
      "v_perm_b32 v232, v15, v23, s31\n\t"
      "v_cvt_pk_bf16_f32 v242, v236, v237\n\t"
      "v_cvt_pk_bf16_f32 v246, v238, v239\n\t"
      "v_perm_b32 v233, v7, v3, s84\n\t"
      "v_perm_b32 v234, v19, v11, s84\n\t"
      "v_perm_b32 v235, v15, v23, s84\n\t"
      "v_dot2_f32_bf16 v236, v230, v231, 0 neg_hi:[1,0,0]\n\t"
      "v_dot2_f32_bf16 v238, v230, v232, 0\n\t"
      "v_dot2_f32_bf16 v237, v233, v234, 0 neg_hi:[1,0,0]\n\t"
      "v_dot2_f32_bf16 v239, v233, v235, 0\n\t"
      "s_nop 1\n\t"
      "v_cvt_pk_bf16_f32 v243, v236, v237\n\t"
      "v_cvt_pk_bf16_f32 v247, v238, v239\n\t"
      "ds_write_b128 v252, v[240:243] offset:0\n\t"
      "ds_write_b128 v253, v[244:247] offset:0\n\t"
      "s_add_u32 s88, s97, 8192\n\t"
      "s_and_b32 s88, s88, 0x3fff\n\t"
      "buffer_load_dwordx4 a[160:163], v154, s[72:75], s88 offen offset:0 nt\n\t"
      "buffer_load_dwordx4 a[164:167], v154, s[72:75], s88 offen offset:128 nt\n\t"
      "buffer_load_dwordx4 a[8:11], v154, s[76:79], s88 offen offset:0\n\t"
      "buffer_load_dwordx4 a[24:27], v154, s[76:79], s88 offen offset:128\n\t"
      "buffer_load_dwordx4 a[40:43], v154, s[80:83], s88 offen offset:0\n\t"
      "buffer_load_dwordx4 a[56:59], v154, s[80:83], s88 offen offset:128\n\t"
      "s_waitcnt vmcnt(19)\n\t"
      "v_perm_b32 v230, v28, v24, s31\n\t"
      "v_perm_b32 v231, v40, v32, s31\n\t"
      "v_perm_b32 v232, v36, v44, s31\n\t"
      "v_perm_b32 v233, v28, v24, s84\n\t"
      "v_perm_b32 v234, v40, v32, s84\n\t"
      "v_perm_b32 v235, v36, v44, s84\n\t"
      "v_dot2_f32_bf16 v236, v230, v231, 0 neg_hi:[1,0,0]\n\t"
      "v_dot2_f32_bf16 v238, v230, v232, 0\n\t"
      "v_dot2_f32_bf16 v237, v233, v234, 0 neg_hi:[1,0,0]\n\t"
      "v_dot2_f32_bf16 v239, v233, v235, 0\n\t"
      "v_perm_b32 v230, v29, v25, s31\n\t"
      "v_perm_b32 v231, v41, v33, s31\n\t"
      "v_perm_b32 v232, v37, v45, s31\n\t"
      "v_cvt_pk_bf16_f32 v240, v236, v237\n\t"
      "v_cvt_pk_bf16_f32 v244, v238, v239\n\t"
      "v_perm_b32 v233, v29, v25, s84\n\t"
      "v_perm_b32 v234, v41, v33, s84\n\t"
      "v_perm_b32 v235, v37, v45, s84\n\t"
      "v_dot2_f32_bf16 v236, v230, v231, 0 neg_hi:[1,0,0]\n\t"
      "v_dot2_f32_bf16 v238, v230, v232, 0\n\t"
      "v_dot2_f32_bf16 v237, v233, v234, 0 neg_hi:[1,0,0]\n\t"
      "v_dot2_f32_bf16 v239, v233, v235, 0\n\t"
      "v_perm_b32 v230, v30, v26, s31\n\t"
      "v_perm_b32 v231, v42, v34, s31\n\t"
      "v_perm_b32 v232, v38, v46, s31\n\t"
      "v_cvt_pk_bf16_f32 v241, v236, v237\n\t"
      "v_cvt_pk_bf16_f32 v245, v238, v239\n\t"
      "v_perm_b32 v233, v30, v26, s84\n\t"
      "v_perm_b32 v234, v42, v34, s84\n\t"
      "v_perm_b32 v235, v38, v46, s84\n\t"
      "v_dot2_f32_bf16 v236, v230, v231, 0 neg_hi:[1,0,0]\n\t"
      "v_dot2_f32_bf16 v238, v230, v232, 0\n\t"
      "v_dot2_f32_bf16 v237, v233, v234, 0 neg_hi:[1,0,0]\n\t"
      "v_dot2_f32_bf16 v239, v233, v235, 0\n\t"
      "v_perm_b32 v230, v31, v27, s31\n\t"
      "v_perm_b32 v231, v43, v35, s31\n\t"
      "v_perm_b32 v232, v39, v47, s31\n\t"
      "v_cvt_pk_bf16_f32 v242, v236, v237\n\t"
      "v_cvt_pk_bf16_f32 v246, v238, v239\n\t"
      "v_perm_b32 v233, v31, v27, s84\n\t"
      "v_perm_b32 v234, v43, v35, s84\n\t"
      "v_perm_b32 v235, v39, v47, s84\n\t"
      "v_dot2_f32_bf16 v236, v230, v231, 0 neg_hi:[1,0,0]\n\t"
      "v_dot2_f32_bf16 v238, v230, v232, 0\n\t"
      "v_dot2_f32_bf16 v237, v233, v234, 0 neg_hi:[1,0,0]\n\t"
      "v_dot2_f32_bf16 v239, v233, v235, 0\n\t"
      "s_nop 1\n\t"
      "v_cvt_pk_bf16_f32 v243, v236, v237\n\t"
      "v_cvt_pk_bf16_f32 v247, v238, v239\n\t"
      "ds_write_b128 v253, v[240:243] offset:2048\n\t"
      "ds_write_b128 v252, v[244:247] offset:2048\n\t"
      "ds_read_b128 a[128:131], v148\n\t"
      "ds_read_b128 a[132:135], v149\n\t"
      "ds_read_b128 a[136:139], v150\n\t"
      "ds_read_b128 a[140:143], v151\n\t"
      "buffer_load_dwordx2 v[166:167], v162, s[36:39], s30 offen sc1\n\t"
      "s_waitcnt vmcnt(14)\n\t"
      "v_perm_b32 v230, v52, v48, s31\n\t"
      "s_add_u32 s88, s97, 10240\n\t"
      "s_and_b32 s88, s88, 0x3fff\n\t"
      "buffer_load_dwordx4 a[168:171], v154, s[72:75], s88 offen offset:0 nt\n\t"
      "v_perm_b32 v231, v64, v56, s31\n\t"
      "v_perm_b32 v232, v60, v68, s31\n\t"
      "v_perm_b32 v233, v52, v48, s84\n\t"
      "v_perm_b32 v234, v64, v56, s84\n\t"
      "v_perm_b32 v235, v60, v68, s84\n\t"
      "v_dot2_f32_bf16 v236, v230, v231, 0 neg_hi:[1,0,0]\n\t"
      "v_dot2_f32_bf16 v238, v230, v232, 0\n\t"
      "v_dot2_f32_bf16 v237, v233, v234, 0 neg_hi:[1,0,0]\n\t"
      "buffer_load_dwordx4 a[172:175], v154, s[72:75], s88 offen offset:128 nt\n\t"
      "v_dot2_f32_bf16 v239, v233, v235, 0\n\t"
      "v_perm_b32 v230, v53, v49, s31\n\t"
      "v_perm_b32 v231, v65, v57, s31\n\t"
      "v_perm_b32 v232, v61, v69, s31\n\t"
      "v_cvt_pk_bf16_f32 v240, v236, v237\n\t"
      "v_cvt_pk_bf16_f32 v244, v238, v239\n\t"
      "v_perm_b32 v233, v53, v49, s84\n\t"
      "v_perm_b32 v234, v65, v57, s84\n\t"
      "buffer_load_dwordx4 a[72:75], v154, s[76:79], s88 offen offset:0\n\t"
      "v_perm_b32 v235, v61, v69, s84\n\t"
      "v_dot2_f32_bf16 v236, v230, v231, 0 neg_hi:[1,0,0]\n\t"
      "v_dot2_f32_bf16 v238, v230, v232, 0\n\t"
      "v_dot2_f32_bf16 v237, v233, v234, 0 neg_hi:[1,0,0]\n\t"
      "v_dot2_f32_bf16 v239, v233, v235, 0\n\t"
      "v_perm_b32 v230, v54, v50, s31\n\t"
      "v_perm_b32 v231, v66, v58, s31\n\t"
      "v_perm_b32 v232, v62, v70, s31\n\t"
      "buffer_load_dwordx4 a[88:91], v154, s[76:79], s88 offen offset:128\n\t"
      "v_cvt_pk_bf16_f32 v241, v236, v237\n\t"
      "v_cvt_pk_bf16_f32 v245, v238, v239\n\t"
      "v_perm_b32 v233, v54, v50, s84\n\t"
      "v_perm_b32 v234, v66, v58, s84\n\t"
      "v_perm_b32 v235, v62, v70, s84\n\t"
      "v_dot2_f32_bf16 v236, v230, v231, 0 neg_hi:[1,0,0]\n\t"
      "v_dot2_f32_bf16 v238, v230, v232, 0\n\t"
      "v_dot2_f32_bf16 v237, v233, v234, 0 neg_hi:[1,0,0]\n\t"
      "buffer_load_dwordx4 a[104:107], v154, s[80:83], s88 offen offset:0\n\t"
      "v_dot2_f32_bf16 v239, v233, v235, 0\n\t"
      "v_perm_b32 v230, v55, v51, s31\n\t"
      "v_perm_b32 v231, v67, v59, s31\n\t"
      "v_perm_b32 v232, v63, v71, s31\n\t"
      "v_cvt_pk_bf16_f32 v242, v236, v237\n\t"
      "v_cvt_pk_bf16_f32 v246, v238, v239\n\t"
      "v_perm_b32 v233, v55, v51, s84\n\t"
      "v_perm_b32 v234, v67, v59, s84\n\t"
      "buffer_load_dwordx4 a[120:123], v154, s[80:83], s88 offen offset:128\n\t"
      "v_perm_b32 v235, v63, v71, s84\n\t"
      "v_dot2_f32_bf16 v236, v230, v231, 0 neg_hi:[1,0,0]\n\t"
      "v_dot2_f32_bf16 v238, v230, v232, 0\n\t"
      "v_dot2_f32_bf16 v237, v233, v234, 0 neg_hi:[1,0,0]\n\t"
      "v_dot2_f32_bf16 v239, v233, v235, 0\n\t"
      "s_nop 1\n\t"
      "v_cvt_pk_bf16_f32 v243, v236, v237\n\t"
      "v_cvt_pk_bf16_f32 v247, v238, v239\n\t"
      "ds_write_b128 v252, v[240:243] offset:0\n\t"
      "ds_write_b128 v253, v[244:247] offset:0\n\t"
      "s_waitcnt vmcnt(13)\n\t"
      "v_perm_b32 v230, v76, v72, s31\n\t"
      "v_perm_b32 v231, v88, v80, s31\n\t"
      "v_perm_b32 v232, v84, v92, s31\n\t"
      "v_perm_b32 v233, v76, v72, s84\n\t"
      "v_perm_b32 v234, v88, v80, s84\n\t"
      "v_perm_b32 v235, v84, v92, s84\n\t"
      "v_dot2_f32_bf16 v236, v230, v231, 0 neg_hi:[1,0,0]\n\t"
      "v_dot2_f32_bf16 v238, v230, v232, 0\n\t"
      "v_dot2_f32_bf16 v237, v233, v234, 0 neg_hi:[1,0,0]\n\t"
      "v_dot2_f32_bf16 v239, v233, v235, 0\n\t"
      "v_perm_b32 v230, v77, v73, s31\n\t"
      "v_perm_b32 v231, v89, v81, s31\n\t"
      "v_perm_b32 v232, v85, v93, s31\n\t"
      "v_cvt_pk_bf16_f32 v240, v236, v237\n\t"
      "v_cvt_pk_bf16_f32 v244, v238, v239\n\t"
      "v_perm_b32 v233, v77, v73, s84\n\t"
      "v_perm_b32 v234, v89, v81, s84\n\t"
      "v_perm_b32 v235, v85, v93, s84\n\t"
      "v_dot2_f32_bf16 v236, v230, v231, 0 neg_hi:[1,0,0]\n\t"
      "v_dot2_f32_bf16 v238, v230, v232, 0\n\t"
      "v_dot2_f32_bf16 v237, v233, v234, 0 neg_hi:[1,0,0]\n\t"
      "v_dot2_f32_bf16 v239, v233, v235, 0\n\t"
      "v_perm_b32 v230, v78, v74, s31\n\t"
      "v_perm_b32 v231, v90, v82, s31\n\t"
      "v_perm_b32 v232, v86, v94, s31\n\t"
      "v_cvt_pk_bf16_f32 v241, v236, v237\n\t"
      "v_cvt_pk_bf16_f32 v245, v238, v239\n\t"
      "v_perm_b32 v233, v78, v74, s84\n\t"
      "v_perm_b32 v234, v90, v82, s84\n\t"
      "v_perm_b32 v235, v86, v94, s84\n\t"
      "v_dot2_f32_bf16 v236, v230, v231, 0 neg_hi:[1,0,0]\n\t"
      "v_dot2_f32_bf16 v238, v230, v232, 0\n\t"
      "v_dot2_f32_bf16 v237, v233, v234, 0 neg_hi:[1,0,0]\n\t"
      "v_dot2_f32_bf16 v239, v233, v235, 0\n\t"
      "v_perm_b32 v230, v79, v75, s31\n\t"
      "v_perm_b32 v231, v91, v83, s31\n\t"
      "v_perm_b32 v232, v87, v95, s31\n\t"
      "v_cvt_pk_bf16_f32 v242, v236, v237\n\t"
      "v_cvt_pk_bf16_f32 v246, v238, v239\n\t"
      "v_perm_b32 v233, v79, v75, s84\n\t"
      "v_perm_b32 v234, v91, v83, s84\n\t"
      "v_perm_b32 v235, v87, v95, s84\n\t"
      "v_dot2_f32_bf16 v236, v230, v231, 0 neg_hi:[1,0,0]\n\t"
      "v_dot2_f32_bf16 v238, v230, v232, 0\n\t"
      "v_dot2_f32_bf16 v237, v233, v234, 0 neg_hi:[1,0,0]\n\t"
      "v_dot2_f32_bf16 v239, v233, v235, 0\n\t"
      "s_nop 1\n\t"
      "v_cvt_pk_bf16_f32 v243, v236, v237\n\t"
      "v_cvt_pk_bf16_f32 v247, v238, v239\n\t"
      "ds_write_b128 v253, v[240:243] offset:2048\n\t"
      "ds_write_b128 v252, v[244:247] offset:2048\n\t"
      "ds_read_b128 a[144:147], v148\n\t"
      "ds_read_b128 a[148:151], v149\n\t"
      "ds_read_b128 a[152:155], v150\n\t"
      "ds_read_b128 a[156:159], v151\n\t"
      "s_waitcnt vmcnt(6)\n\t"
      "v_cmp_ne_u64 vcc, v[166:167], v[164:165]\n\t"
      "s_nop 1\n\t"
      "s_cbranch_vccnz .Lspin_pro\n\t"
      ".Lback_pro:\n\t"
      "s_add_u32 m0, s85, 0\n\t"
      "s_nop 0\n\t"
      "buffer_load_dwordx4 v152, s[36:39], s24 offen offset:0 sc1 lds\n\t"
      "buffer_load_dwordx4 v152, s[36:39], s24 offen offset:1024 sc1 lds\n\t"
      "buffer_load_dwordx4 v152, s[36:39], s24 offen offset:2048 sc1 lds\n\t"
      "buffer_load_dwordx4 v152, s[36:39], s24 offen offset:3072 sc1 lds\n\t"
      "s_add_u32 s24, s24, 4096\n\t"
      "s_add_u32 m0, s85, 4096\n\t"
      "s_nop 0\n\t"
      "buffer_load_dwordx4 v152, s[36:39], s24 offen offset:0 sc1 lds\n\t"
      "buffer_load_dwordx4 v152, s[36:39], s24 offen offset:1024 sc1 lds\n\t"
      "buffer_load_dwordx4 v152, s[36:39], s24 offen offset:2048 sc1 lds\n\t"
      "buffer_load_dwordx4 v152, s[36:39], s24 offen offset:3072 sc1 lds\n\t"
      "s_add_u32 s24, s24, 126976\n\t"
      "s_waitcnt vmcnt(15)\n\t"
      "v_accvgpr_read_b32 v224, a160\n\t"
      "buffer_load_dwordx4 v[170:173], v153, s[40:43], s90 offen offset:0\n\t"
      "v_accvgpr_read_b32 v225, a164\n\t"
      "v_accvgpr_read_b32 v226, a8\n\t"
      "v_accvgpr_read_b32 v227, a24\n\t"
      "buffer_load_dwordx4 v[174:177], v153, s[40:43], s90 offen offset:16\n\t"
      "v_accvgpr_read_b32 v228, a40\n\t"
      "v_accvgpr_read_b32 v229, a56\n\t"
      "v_perm_b32 v230, v225, v224, s31\n\t"
      "v_perm_b32 v231, v228, v226, s31\n\t"
      "buffer_load_dwordx4 v[178:181], v153, s[40:43], s90 offen offset:256\n\t"
      "v_perm_b32 v232, v227, v229, s31\n\t"
      "v_perm_b32 v233, v225, v224, s84\n\t"
      "v_perm_b32 v234, v228, v226, s84\n\t"
      "buffer_load_dwordx4 v[182:185], v153, s[40:43], s90 offen offset:272\n\t"
      "v_perm_b32 v235, v227, v229, s84\n\t"
      "v_dot2_f32_bf16 v236, v230, v231, 0 neg_hi:[1,0,0]\n\t"
      "v_dot2_f32_bf16 v238, v230, v232, 0\n\t"
      "v_dot2_f32_bf16 v237, v233, v234, 0 neg_hi:[1,0,0]\n\t"
      "buffer_load_dwordx2 v[206:207], v159, s[60:63], s90 offen offset:0 nt\n\t"
      "v_dot2_f32_bf16 v239, v233, v235, 0\n\t"
      "v_accvgpr_read_b32 v224, a161\n\t"
      "v_accvgpr_read_b32 v225, a165\n\t"
      "v_accvgpr_read_b32 v226, a9\n\t"
      "buffer_load_dwordx2 v[208:209], v159, s[60:63], s90 offen offset:512 nt\n\t"
      "v_accvgpr_read_b32 v227, a25\n\t"
      "v_accvgpr_read_b32 v228, a41\n\t"
      "v_accvgpr_read_b32 v229, a57\n\t"
      "buffer_load_dwordx2 v[210:211], v159, s[60:63], s90 offen offset:1024 nt\n\t"
      "v_cvt_pk_bf16_f32 v240, v236, v237\n\t"
      "v_cvt_pk_bf16_f32 v244, v238, v239\n\t"
      "v_perm_b32 v230, v225, v224, s31\n\t"
      "v_perm_b32 v231, v228, v226, s31\n\t"
      "buffer_load_dwordx2 v[212:213], v159, s[60:63], s90 offen offset:1536 nt\n\t"
      "v_perm_b32 v232, v227, v229, s31\n\t"
      "v_perm_b32 v233, v225, v224, s84\n\t"
      "v_perm_b32 v234, v228, v226, s84\n\t"
      "buffer_load_dwordx2 v[214:215], v159, s[60:63], s90 offen offset:2048 nt\n\t"
      "v_perm_b32 v235, v227, v229, s84\n\t"
      "v_dot2_f32_bf16 v236, v230, v231, 0 neg_hi:[1,0,0]\n\t"
      "v_dot2_f32_bf16 v238, v230, v232, 0\n\t"
      "v_dot2_f32_bf16 v237, v233, v234, 0 neg_hi:[1,0,0]\n\t"
      "buffer_load_dwordx2 v[216:217], v159, s[60:63], s90 offen offset:2560 nt\n\t"
      "v_dot2_f32_bf16 v239, v233, v235, 0\n\t"
      "v_accvgpr_read_b32 v224, a162\n\t"
      "v_accvgpr_read_b32 v225, a166\n\t"
      "v_accvgpr_read_b32 v226, a10\n\t"
      "buffer_load_dwordx2 v[218:219], v159, s[60:63], s90 offen offset:3072 nt\n\t"
      "v_accvgpr_read_b32 v227, a26\n\t"
      "v_accvgpr_read_b32 v228, a42\n\t"
      "v_accvgpr_read_b32 v229, a58\n\t"
      "buffer_load_dwordx2 v[220:221], v159, s[60:63], s90 offen offset:3584 nt\n\t"
      "v_cvt_pk_bf16_f32 v241, v236, v237\n\t"
      "v_cvt_pk_bf16_f32 v245, v238, v239\n\t"
      "v_perm_b32 v230, v225, v224, s31\n\t"
      "v_perm_b32 v231, v228, v226, s31\n\t"
      "buffer_load_dwordx2 v[202:203], v155, s[52:55], s92 offen\n\t"
      "v_perm_b32 v232, v227, v229, s31\n\t"
      "v_perm_b32 v233, v225, v224, s84\n\t"
      "v_perm_b32 v234, v228, v226, s84\n\t"
      "buffer_load_dwordx2 v[204:205], v155, s[56:59], s92 offen\n\t"
      "v_perm_b32 v235, v227, v229, s84\n\t"
      "v_dot2_f32_bf16 v236, v230, v231, 0 neg_hi:[1,0,0]\n\t"
      "v_dot2_f32_bf16 v238, v230, v232, 0\n\t"
      "v_dot2_f32_bf16 v237, v233, v234, 0 neg_hi:[1,0,0]\n\t"
      "buffer_load_ushort v222, v160, s[64:67], s92 offen\n\t"
      "v_dot2_f32_bf16 v239, v233, v235, 0\n\t"
      "v_accvgpr_read_b32 v224, a163\n\t"
      "v_accvgpr_read_b32 v225, a167\n\t"
      "v_accvgpr_read_b32 v226, a11\n\t"
      "buffer_load_ushort v223, v160, s[68:71], s92 offen\n\t"
      "v_accvgpr_read_b32 v227, a27\n\t"
      "v_accvgpr_read_b32 v228, a43\n\t"
      "v_accvgpr_read_b32 v229, a59\n\t"
      "buffer_load_dwordx4 v[186:189], v154, s[44:47], s91 offen offset:0\n\t"
      "v_cvt_pk_bf16_f32 v242, v236, v237\n\t"
      "v_cvt_pk_bf16_f32 v246, v238, v239\n\t"
      "v_perm_b32 v230, v225, v224, s31\n\t"
      "v_perm_b32 v231, v228, v226, s31\n\t"
      "buffer_load_dwordx4 v[190:193], v154, s[44:47], s91 offen offset:128\n\t"
      "v_perm_b32 v232, v227, v229, s31\n\t"
      "v_perm_b32 v233, v225, v224, s84\n\t"
      "v_perm_b32 v234, v228, v226, s84\n\t"
      "buffer_load_dwordx4 v[194:197], v154, s[48:51], s91 offen offset:0\n\t"
      "v_perm_b32 v235, v227, v229, s84\n\t"
      "v_dot2_f32_bf16 v236, v230, v231, 0 neg_hi:[1,0,0]\n\t"
      "v_dot2_f32_bf16 v238, v230, v232, 0\n\t"
      "v_dot2_f32_bf16 v237, v233, v234, 0 neg_hi:[1,0,0]\n\t"
      "buffer_load_dwordx4 v[198:201], v154, s[48:51], s91 offen offset:128\n\t"
      "s_add_u32 s90, s90, 131072\n\t"
      "s_add_u32 s91, s91, 65536\n\t"
      "s_add_u32 s92, s92, 2048\n\t"
      "v_dot2_f32_bf16 v239, v233, v235, 0\n\t"
      "v_cvt_pk_bf16_f32 v243, v236, v237\n\t"
      "s_nop 1\n\t"
      "v_cvt_pk_bf16_f32 v247, v238, v239\n\t"
      "ds_write_b128 v252, v[240:243] offset:0\n\t"
      "ds_write_b128 v253, v[244:247] offset:0\n\t"
      "s_waitcnt vmcnt(20)\n\t"
      "s_barrier\n\t"
      "ds_read_b128 a[192:195], v136 offset:0\n\t"
      "ds_read_b128 a[196:199], v137 offset:0\n\t"
      "ds_read_b128 a[200:203], v138 offset:0\n\t"
      "ds_read_b128 a[204:207], v139 offset:0\n\t"
      "ds_read_b128 a[208:211], v136 offset:4096\n\t"
      "ds_read_b128 a[212:215], v137 offset:4096\n\t"
      "ds_read_b128 a[216:219], v138 offset:4096\n\t"
      "ds_read_b128 a[220:223], v139 offset:4096\n\t"
      "ds_read_b128 a[224:227], v136 offset:8192\n\t"
      "ds_read_b128 a[228:231], v137 offset:8192\n\t"
      "ds_read_b128 a[232:235], v138 offset:8192\n\t"
      "ds_read_b128 a[236:239], v139 offset:8192\n\t"
      "ds_read_b128 a[240:243], v136 offset:12288\n\t"
      "ds_read_b128 a[244:247], v137 offset:12288\n\t"
      "ds_read_b128 a[248:251], v138 offset:12288\n\t"
      "ds_read_b128 a[252:255], v139 offset:12288\n\t"
      "s_waitcnt lgkmcnt(4)\n\t"
      "ds_read_b128 v[0:3], v144 offset:16384\n\t"
      "ds_read_b128 v[4:7], v144 offset:24576\n\t"
      "ds_read_b128 v[8:11], v144 offset:17408\n\t"
      "ds_read_b128 v[12:15], v144 offset:25600\n\t"
      "ds_read_b128 v[16:19], v145 offset:16384\n\t"
      "ds_read_b128 v[20:23], v145 offset:24576\n\t"
      "ds_read_b128 v[24:27], v145 offset:17408\n\t"
      "ds_read_b128 v[28:31], v145 offset:25600\n\t"
      "ds_read_b128 v[32:35], v146 offset:16384\n\t"
      "ds_read_b128 v[36:39], v146 offset:24576\n\t"
      "ds_read_b128 v[40:43], v146 offset:17408\n\t"
      "s_waitcnt lgkmcnt(8)\n\t"
      "ds_read_b128 v[44:47], v146 offset:25600\n\t"
      "ds_read_b128 v[48:51], v147 offset:16384\n\t"
      "ds_read_b128 v[52:55], v147 offset:24576\n\t"
      "ds_read_b128 v[56:59], v147 offset:17408\n\t"
      "ds_read_b128 v[60:63], v147 offset:25600\n\t"
      "v_and_b32 v248, 0xf0, v152\n\t"
      "v_cmp_eq_u32 vcc, 0, v248\n\t"
      "v_mov_b32 v249, 0x3f803f80\n\t"
      "s_nop 0\n\t"
      "v_cndmask_b32 v248, 0, v249, vcc\n\t"
      "v_mov_b32 v249, v248\n\t"
      "v_mov_b32 v250, v248\n\t"
      "v_mov_b32 v251, v248\n\t"
      "s_waitcnt lgkmcnt(0)\n\t"
      "v_mfma_f32_16x16x32_bf16 v[112:115], a[192:195], a[128:131], 0\n\t"
      "v_mfma_f32_16x16x32_bf16 v[112:115], a[196:199], a[132:135], v[112:115]\n\t"
      "v_mfma_f32_16x16x32_bf16 v[112:115], a[200:203], a[136:139], v[112:115]\n\t"
      "v_mfma_f32_16x16x32_bf16 v[112:115], a[204:207], a[140:143], v[112:115]\n\t"
      "s_nop 7\n\t"
      "v_max3_f32 v224, v112, v113, v114\n\t"
      "v_max_f32 v224, v224, v115\n\t"
      "v_mov_b32 v225, v224\n\t"
      "s_nop 0\n\t"
      "s_nop 0\n\t"
      "v_permlane16_swap_b32 v224, v225\n\t"
      "v_max_f32 v224, v224, v225\n\t"
      "v_mov_b32 v225, v224\n\t"
      "s_nop 0\n\t"
      "s_nop 0\n\t"
      "v_permlane32_swap_b32 v224, v225\n\t"
      "v_max_f32 v224, v224, v225\n\t"
      "v_sub_f32 v112, 0xc2200000, v224\n\t"
      "v_sub_f32 v113, 0xc2200000, v224\n\t"
      "v_sub_f32 v114, 0xc2200000, v224\n\t"
      "v_sub_f32 v115, 0xc2200000, v224\n\t"
      "s_nop 0\n\t"
      "s_nop 0\n\t"
      "v_mfma_f32_16x16x32_bf16 v[64:67], a[192:195], a[128:131], v[112:115]\n\t"
      "s_add_u32 s88, s97, 12288\n\t"
      "s_and_b32 s88, s88, 0x3fff\n\t"
      "buffer_load_dwordx4 a[176:179], v154, s[72:75], s88 offen offset:0 nt\n\t"
      "v_mfma_f32_16x16x32_bf16 v[64:67], a[196:199], a[132:135], v[64:67]\n\t"
      "v_mfma_f32_16x16x32_bf16 v[64:67], a[200:203], a[136:139], v[64:67]\n\t"
      "buffer_load_dwordx4 a[180:183], v154, s[72:75], s88 offen offset:128 nt\n\t"
      "v_mfma_f32_16x16x32_bf16 v[64:67], a[204:207], a[140:143], v[64:67]\n\t"
      "v_mfma_f32_16x16x32_bf16 v[68:71], a[220:223], a[140:143], v[112:115]\n\t"
      "v_mfma_f32_16x16x32_bf16 v[68:71], a[216:219], a[136:139], v[68:71]\n\t"
      "buffer_load_dwordx4 a[12:15], v154, s[76:79], s88 offen offset:0\n\t"
      "v_mfma_f32_16x16x32_bf16 v[68:71], a[212:215], a[132:135], v[68:71]\n\t"
      "v_mfma_f32_16x16x32_bf16 v[68:71], a[208:211], a[128:131], v[68:71]\n\t"
      "v_mfma_f32_16x16x32_bf16 v[72:75], a[224:227], a[128:131], v[112:115]\n\t"
      "buffer_load_dwordx4 a[28:31], v154, s[76:79], s88 offen offset:128\n\t"
      "s_nop 0\n\t"
      "v_exp_f32 v64, v64\n\t"
      "v_mfma_f32_16x16x32_bf16 v[72:75], a[228:231], a[132:135], v[72:75]\n\t"
      "v_mfma_f32_16x16x32_bf16 v[72:75], a[232:235], a[136:139], v[72:75]\n\t"
      "buffer_load_dwordx4 a[44:47], v154, s[80:83], s88 offen offset:0\n\t"
      "v_exp_f32 v65, v65\n\t"
      "v_mfma_f32_16x16x32_bf16 v[72:75], a[236:239], a[140:143], v[72:75]\n\t"
      "v_cvt_pk_bf16_f32 v96, v64, v65\n\t"
      "v_mfma_f32_16x16x32_bf16 v[76:79], a[252:255], a[140:143], v[112:115]\n\t"
      "v_exp_f32 v66, v66\n\t"
      "v_mfma_f32_16x16x32_bf16 v[76:79], a[248:251], a[136:139], v[76:79]\n\t"
      "buffer_load_dwordx4 a[60:63], v154, s[80:83], s88 offen offset:128\n\t"
      "v_mfma_f32_16x16x32_bf16 v[76:79], a[244:247], a[132:135], v[76:79]\n\t"
      "v_exp_f32 v67, v67\n\t"
      "v_mfma_f32_16x16x32_bf16 v[76:79], a[240:243], a[128:131], v[76:79]\n\t"
      "v_cvt_pk_bf16_f32 v97, v66, v67\n\t"
      "v_exp_f32 v68, v68\n\t"
      "v_exp_f32 v69, v69\n\t"
      "s_nop 0\n\t"
      "v_cvt_pk_bf16_f32 v98, v68, v69\n\t"
      "v_exp_f32 v70, v70\n\t"
      "v_exp_f32 v71, v71\n\t"
      "s_nop 0\n\t"
      "v_cvt_pk_bf16_f32 v99, v70, v71\n\t"
      "v_exp_f32 v72, v72\n\t"
      "v_exp_f32 v73, v73\n\t"
      "s_nop 0\n\t"
      "v_cvt_pk_bf16_f32 v100, v72, v73\n\t"
      "v_exp_f32 v74, v74\n\t"
      "v_exp_f32 v75, v75\n\t"
      "s_nop 0\n\t"
      "v_cvt_pk_bf16_f32 v101, v74, v75\n\t"
      "v_exp_f32 v76, v76\n\t"
      "s_waitcnt vmcnt(34)\n\t"
      "v_accvgpr_read_b32 v224, a168\n\t"
      "v_accvgpr_read_b32 v225, a172\n\t"
      "v_accvgpr_read_b32 v226, a72\n\t"
      "v_accvgpr_read_b32 v227, a88\n\t"
      "v_accvgpr_read_b32 v228, a104\n\t"
      "v_accvgpr_read_b32 v229, a120\n\t"
      "v_perm_b32 v230, v225, v224, s31\n\t"
      "v_perm_b32 v231, v228, v226, s31\n\t"
      "v_perm_b32 v232, v227, v229, s31\n\t"
      "v_perm_b32 v233, v225, v224, s84\n\t"
      "v_perm_b32 v234, v228, v226, s84\n\t"
      "v_perm_b32 v235, v227, v229, s84\n\t"
      "v_dot2_f32_bf16 v236, v230, v231, 0 neg_hi:[1,0,0]\n\t"
      "v_dot2_f32_bf16 v238, v230, v232, 0\n\t"
      "v_dot2_f32_bf16 v237, v233, v234, 0 neg_hi:[1,0,0]\n\t"
      "v_dot2_f32_bf16 v239, v233, v235, 0\n\t"
      "v_accvgpr_read_b32 v224, a169\n\t"
      "v_accvgpr_read_b32 v225, a173\n\t"
      "v_accvgpr_read_b32 v226, a73\n\t"
      "v_accvgpr_read_b32 v227, a89\n\t"
      "v_accvgpr_read_b32 v228, a105\n\t"
      "v_accvgpr_read_b32 v229, a121\n\t"
      "v_cvt_pk_bf16_f32 v240, v236, v237\n\t"
      "v_cvt_pk_bf16_f32 v244, v238, v239\n\t"
      "v_perm_b32 v230, v225, v224, s31\n\t"
      "v_perm_b32 v231, v228, v226, s31\n\t"
      "v_perm_b32 v232, v227, v229, s31\n\t"
      "v_perm_b32 v233, v225, v224, s84\n\t"
      "v_perm_b32 v234, v228, v226, s84\n\t"
      "v_perm_b32 v235, v227, v229, s84\n\t"
      "v_dot2_f32_bf16 v236, v230, v231, 0 neg_hi:[1,0,0]\n\t"
      "v_dot2_f32_bf16 v238, v230, v232, 0\n\t"
      "v_dot2_f32_bf16 v237, v233, v234, 0 neg_hi:[1,0,0]\n\t"
      "v_dot2_f32_bf16 v239, v233, v235, 0\n\t"
      "v_accvgpr_read_b32 v224, a170\n\t"
      "v_accvgpr_read_b32 v225, a174\n\t"
      "v_accvgpr_read_b32 v226, a74\n\t"
      "v_accvgpr_read_b32 v227, a90\n\t"
      "v_accvgpr_read_b32 v228, a106\n\t"
      "v_accvgpr_read_b32 v229, a122\n\t"
      "v_cvt_pk_bf16_f32 v241, v236, v237\n\t"
      "v_cvt_pk_bf16_f32 v245, v238, v239\n\t"
      "v_perm_b32 v230, v225, v224, s31\n\t"
      "v_perm_b32 v231, v228, v226, s31\n\t"
      "v_perm_b32 v232, v227, v229, s31\n\t"
      "v_perm_b32 v233, v225, v224, s84\n\t"
      "v_perm_b32 v234, v228, v226, s84\n\t"
      "v_perm_b32 v235, v227, v229, s84\n\t"
      "v_dot2_f32_bf16 v236, v230, v231, 0 neg_hi:[1,0,0]\n\t"
      "v_dot2_f32_bf16 v238, v230, v232, 0\n\t"
      "v_dot2_f32_bf16 v237, v233, v234, 0 neg_hi:[1,0,0]\n\t"
      "v_dot2_f32_bf16 v239, v233, v235, 0\n\t"
      "v_accvgpr_read_b32 v224, a171\n\t"
      "v_accvgpr_read_b32 v225, a175\n\t"
      "v_accvgpr_read_b32 v226, a75\n\t"
      "v_accvgpr_read_b32 v227, a91\n\t"
      "v_accvgpr_read_b32 v228, a107\n\t"
      "v_accvgpr_read_b32 v229, a123\n\t"
      "v_cvt_pk_bf16_f32 v242, v236, v237\n\t"
      "v_cvt_pk_bf16_f32 v246, v238, v239\n\t"
      "v_perm_b32 v230, v225, v224, s31\n\t"
      "v_perm_b32 v231, v228, v226, s31\n\t"
      "v_perm_b32 v232, v227, v229, s31\n\t"
      "v_perm_b32 v233, v225, v224, s84\n\t"
      "v_perm_b32 v234, v228, v226, s84\n\t"
      "v_perm_b32 v235, v227, v229, s84\n\t"
      "v_dot2_f32_bf16 v236, v230, v231, 0 neg_hi:[1,0,0]\n\t"
      "v_dot2_f32_bf16 v238, v230, v232, 0\n\t"
      "v_dot2_f32_bf16 v237, v233, v234, 0 neg_hi:[1,0,0]\n\t"
      "v_dot2_f32_bf16 v239, v233, v235, 0\n\t"
      "s_nop 1\n\t"
      "v_cvt_pk_bf16_f32 v243, v236, v237\n\t"
      "v_cvt_pk_bf16_f32 v247, v238, v239\n\t"
      "ds_write_b128 v253, v[240:243] offset:2048\n\t"
      "ds_write_b128 v252, v[244:247] offset:2048\n\t"
      "ds_read_b128 a[160:163], v148\n\t"
      "ds_read_b128 a[164:167], v149\n\t"
      "ds_read_b128 a[168:171], v150\n\t"
      "ds_read_b128 a[172:175], v151\n\t"
      "s_waitcnt lgkmcnt(0)\n\t"
      "v_mfma_f32_16x16x32_bf16 v[116:119], a[192:195], a[144:147], 0\n\t"
      "v_mfma_f32_16x16x32_bf16 v[116:119], a[196:199], a[148:151], v[116:119]\n\t"
      "v_mfma_f32_16x16x32_bf16 v[116:119], a[200:203], a[152:155], v[116:119]\n\t"
      "v_mfma_f32_16x16x32_bf16 v[116:119], a[204:207], a[156:159], v[116:119]\n\t"
      "s_nop 7\n\t"
      "v_max3_f32 v224, v116, v117, v118\n\t"
      "v_max_f32 v224, v224, v119\n\t"
      "v_mov_b32 v225, v224\n\t"
      "s_nop 0\n\t"
      "s_nop 0\n\t"
      "v_permlane16_swap_b32 v224, v225\n\t"
      "v_max_f32 v224, v224, v225\n\t"
      "v_mov_b32 v225, v224\n\t"
      "s_nop 0\n\t"
      "s_nop 0\n\t"
      "v_permlane32_swap_b32 v224, v225\n\t"
      "v_max_f32 v224, v224, v225\n\t"
      "v_sub_f32 v116, 0xc2200000, v224\n\t"
      "v_sub_f32 v117, 0xc2200000, v224\n\t"
      "v_sub_f32 v118, 0xc2200000, v224\n\t"
      "v_sub_f32 v119, 0xc2200000, v224\n\t"
      "s_nop 0\n\t"
      "s_nop 0\n\t"
      "v_mfma_f32_16x16x32_bf16 v[80:83], a[192:195], a[144:147], v[116:119]\n\t"
      "s_add_u32 s88, s97, 14336\n\t"
      "s_and_b32 s88, s88, 0x3fff\n\t"
      "buffer_load_dwordx4 a[184:187], v154, s[72:75], s88 offen offset:0 nt\n\t"
      "v_exp_f32 v77, v77\n\t"
      "v_mfma_f32_16x16x32_bf16 v[80:83], a[196:199], a[148:151], v[80:83]\n\t"
      "v_cvt_pk_bf16_f32 v102, v76, v77\n\t"
      "v_mfma_f32_16x16x32_bf16 v[80:83], a[200:203], a[152:155], v[80:83]\n\t"
      "v_exp_f32 v78, v78\n\t"
      "v_mfma_f32_16x16x32_bf16 v[80:83], a[204:207], a[156:159], v[80:83]\n\t"
      "v_mfma_f32_16x16x32_bf16 v[84:87], a[220:223], a[156:159], v[116:119]\n\t"
      "v_exp_f32 v79, v79\n\t"
      "v_mfma_f32_16x16x32_bf16 v[84:87], a[216:219], a[152:155], v[84:87]\n\t"
      "buffer_load_dwordx4 a[188:191], v154, s[72:75], s88 offen offset:128 nt\n\t"
      "v_cvt_pk_bf16_f32 v103, v78, v79\n\t"
      "v_mfma_f32_16x16x32_bf16 v[84:87], a[212:215], a[148:151], v[84:87]\n\t"
      "v_mfma_f32_16x16x32_bf16 v[84:87], a[208:211], a[144:147], v[84:87]\n\t"
      "v_mfma_f32_16x16x32_bf16 v[88:91], a[224:227], a[144:147], v[116:119]\n\t"
      "v_exp_f32 v80, v80\n\t"
      "v_mfma_f32_16x16x32_bf16 v[88:91], a[228:231], a[148:151], v[88:91]\n\t"
      "v_mfma_f32_16x16x32_bf16 v[88:91], a[232:235], a[152:155], v[88:91]\n\t"
      "v_exp_f32 v81, v81\n\t"
      "v_mfma_f32_16x16x32_bf16 v[88:91], a[236:239], a[156:159], v[88:91]\n\t"
      "buffer_load_dwordx4 a[76:79], v154, s[76:79], s88 offen offset:0\n\t"
      "v_cvt_pk_bf16_f32 v104, v80, v81\n\t"
      "v_mfma_f32_16x16x32_bf16 v[92:95], a[252:255], a[156:159], v[116:119]\n\t"
      "v_exp_f32 v82, v82\n\t"
      "v_mfma_f32_16x16x32_bf16 v[92:95], a[248:251], a[152:155], v[92:95]\n\t"
      "v_mfma_f32_16x16x32_bf16 v[92:95], a[244:247], a[148:151], v[92:95]\n\t"
      "v_exp_f32 v83, v83\n\t"
      "v_mfma_f32_16x16x32_bf16 v[92:95], a[240:243], a[144:147], v[92:95]\n\t"
      "v_cvt_pk_bf16_f32 v105, v82, v83\n\t"
      "v_mfma_f32_16x16x32_bf16 a[0:3], v[96:99], v[0:3], 0\n\t"
      "v_exp_f32 v84, v84\n\t"
      "v_mfma_f32_16x16x32_bf16 a[0:3], v[100:103], v[4:7], a[0:3]\n\t"
      "buffer_load_dwordx4 a[92:95], v154, s[76:79], s88 offen offset:128\n\t"
      "v_mfma_f32_16x16x32_bf16 a[16:19], v[100:103], v[12:15], 0\n\t"
      "v_exp_f32 v85, v85\n\t"
      "v_mfma_f32_16x16x32_bf16 a[16:19], v[96:99], v[8:11], a[16:19]\n\t"
      "v_cvt_pk_bf16_f32 v106, v84, v85\n\t"
      "v_mfma_f32_16x16x32_bf16 a[32:35], v[96:99], v[16:19], 0\n\t"
      "v_exp_f32 v86, v86\n\t"
      "v_mfma_f32_16x16x32_bf16 a[32:35], v[100:103], v[20:23], a[32:35]\n\t"
      "v_mfma_f32_16x16x32_bf16 a[48:51], v[100:103], v[28:31], 0\n\t"
      "buffer_load_dwordx4 a[108:111], v154, s[80:83], s88 offen offset:0\n\t"
      "v_exp_f32 v87, v87\n\t"
      "v_mfma_f32_16x16x32_bf16 a[48:51], v[96:99], v[24:27], a[48:51]\n\t"
      "v_cvt_pk_bf16_f32 v107, v86, v87\n\t"
      "v_mfma_f32_16x16x32_bf16 a[64:67], v[96:99], v[32:35], 0\n\t"
      "v_exp_f32 v88, v88\n\t"
      "v_mfma_f32_16x16x32_bf16 a[64:67], v[100:103], v[36:39], a[64:67]\n\t"
      "v_mfma_f32_16x16x32_bf16 a[80:83], v[100:103], v[44:47], 0\n\t"
      "v_exp_f32 v89, v89\n\t"
      "v_mfma_f32_16x16x32_bf16 a[80:83], v[96:99], v[40:43], a[80:83]\n\t"
      "v_cvt_pk_bf16_f32 v108, v88, v89\n\t"
      "v_mfma_f32_16x16x32_bf16 a[96:99], v[96:99], v[48:51], 0\n\t"
      "buffer_load_dwordx4 a[124:127], v154, s[80:83], s88 offen offset:128\n\t"
      "v_exp_f32 v90, v90\n\t"
      "v_mfma_f32_16x16x32_bf16 a[96:99], v[100:103], v[52:55], a[96:99]\n\t"
      "v_mfma_f32_16x16x32_bf16 a[112:115], v[100:103], v[60:63], 0\n\t"
      "v_exp_f32 v91, v91\n\t"
      "v_mfma_f32_16x16x32_bf16 a[112:115], v[96:99], v[56:59], a[112:115]\n\t"
      "v_cvt_pk_bf16_f32 v109, v90, v91\n\t"
      "v_mfma_f32_16x16x32_bf16 v[128:131], v[96:99], v[248:251], 0\n\t"
      "v_exp_f32 v92, v92\n\t"
      "v_mfma_f32_16x16x32_bf16 v[128:131], v[100:103], v[248:251], v[128:131]\n\t"
      "s_waitcnt vmcnt(12)\n\t"
      "v_perm_b32 v224, v202, v202, v156\n\t"
      "s_add_u32 m0, s85, 32768\n\t"
      "s_nop 0\n\t"
      "buffer_load_dwordx4 v152, s[36:39], s25 offen offset:0 sc1 lds\n\t"
      "v_perm_b32 v226, v203, v203, v156\n\t"
      "v_perm_b32 v228, v204, v204, v156\n\t"
      "v_perm_b32 v230, v205, v205, v156\n\t"
      "v_mul_f32 v224, s95, v224\n\t"
      "v_mul_f32 v226, s95, v226\n\t"
      "v_mul_f32_e64 v228, -v228, v224\n\t"
      "v_mul_f32_e64 v230, -v230, v226\n\t"
      "v_cvt_f32_i32 v170, v170\n\t"
      "v_cvt_f32_i32 v171, v171\n\t"
      "v_cvt_f32_i32 v172, v172\n\t"
      "v_cvt_f32_i32 v173, v173\n\t"
      "v_cvt_f32_i32 v174, v174\n\t"
      "v_cvt_f32_i32 v175, v175\n\t"
      "v_cvt_f32_i32 v176, v176\n\t"
      "v_cvt_f32_i32 v177, v177\n\t"
      "v_cvt_f32_i32 v178, v178\n\t"
      "v_cvt_f32_i32 v179, v179\n\t"
      "buffer_load_dwordx4 v152, s[36:39], s25 offen offset:1024 sc1 lds\n\t"
      "v_cvt_f32_i32 v180, v180\n\t"
      "v_cvt_f32_i32 v181, v181\n\t"
      "v_cvt_f32_i32 v182, v182\n\t"
      "v_cvt_f32_i32 v183, v183\n\t"
      "v_cvt_f32_i32 v184, v184\n\t"
      "v_cvt_f32_i32 v185, v185\n\t"
      "v_pk_fma_f32 v[170:171], v[170:171], v[224:225], v[228:229] op_sel_hi:[1,0,0]\n\t"
      "v_pk_fma_f32 v[172:173], v[172:173], v[224:225], v[228:229] op_sel_hi:[1,0,0]\n\t"
      "v_pk_fma_f32 v[174:175], v[174:175], v[224:225], v[228:229] op_sel_hi:[1,0,0]\n\t"
      "v_pk_fma_f32 v[176:177], v[176:177], v[224:225], v[228:229] op_sel_hi:[1,0,0]\n\t"
      "v_pk_fma_f32 v[178:179], v[178:179], v[226:227], v[230:231] op_sel_hi:[1,0,0]\n\t"
      "v_pk_fma_f32 v[180:181], v[180:181], v[226:227], v[230:231] op_sel_hi:[1,0,0]\n\t"
      "v_pk_fma_f32 v[182:183], v[182:183], v[226:227], v[230:231] op_sel_hi:[1,0,0]\n\t"
      "v_pk_fma_f32 v[184:185], v[184:185], v[226:227], v[230:231] op_sel_hi:[1,0,0]\n\t"
      "v_lshlrev_b32 v232, 16, v186\n\t"
      "v_and_b32 v233, s89, v186\n\t"
      "v_lshlrev_b32 v234, 16, v190\n\t"
      "buffer_load_dwordx4 v152, s[36:39], s25 offen offset:2048 sc1 lds\n\t"
      "v_and_b32 v235, s89, v190\n\t"
      "v_lshlrev_b32 v236, 16, v194\n\t"
      "v_and_b32 v237, s89, v194\n\t"
      "v_lshlrev_b32 v238, 16, v198\n\t"
      "v_and_b32 v239, s89, v198\n\t"
      "v_pk_mul_f32 v[232:233], v[170:171], v[232:233]\n\t"
      "v_pk_mul_f32 v[234:235], v[178:179], v[234:235]\n\t"
      "v_pk_fma_f32 v[232:233], v[178:179], v[236:237], v[232:233] neg_lo:[1,0,0] neg_hi:[1,0,0]\n\t"
      "v_pk_fma_f32 v[234:235], v[170:171], v[238:239], v[234:235]\n\t"
      "v_cvt_pk_bf16_f32 v240, v232, v233\n\t"
      "v_cvt_pk_bf16_f32 v244, v234, v235\n\t"
      "v_lshlrev_b32 v232, 16, v187\n\t"
      "v_and_b32 v233, s89, v187\n\t"
      "v_lshlrev_b32 v234, 16, v191\n\t"
      "v_and_b32 v235, s89, v191\n\t"
      "v_lshlrev_b32 v236, 16, v195\n\t"
      "v_and_b32 v237, s89, v195\n\t"
      "v_lshlrev_b32 v238, 16, v199\n\t"
      "buffer_load_dwordx4 v152, s[36:39], s25 offen offset:3072 sc1 lds\n\t"
      "v_and_b32 v239, s89, v199\n\t"
      "v_pk_mul_f32 v[232:233], v[172:173], v[232:233]\n\t"
      "v_pk_mul_f32 v[234:235], v[180:181], v[234:235]\n\t"
      "v_pk_fma_f32 v[232:233], v[180:181], v[236:237], v[232:233] neg_lo:[1,0,0] neg_hi:[1,0,0]\n\t"
      "v_pk_fma_f32 v[234:235], v[172:173], v[238:239], v[234:235]\n\t"
      "v_cvt_pk_bf16_f32 v241, v232, v233\n\t"
      "v_cvt_pk_bf16_f32 v245, v234, v235\n\t"
      "v_lshlrev_b32 v232, 16, v188\n\t"
      "v_and_b32 v233, s89, v188\n\t"
      "v_lshlrev_b32 v234, 16, v192\n\t"
      "v_and_b32 v235, s89, v192\n\t"
      "v_lshlrev_b32 v236, 16, v196\n\t"
      "v_and_b32 v237, s89, v196\n\t"
      "v_lshlrev_b32 v238, 16, v200\n\t"
      "v_and_b32 v239, s89, v200\n\t"
      "v_pk_mul_f32 v[232:233], v[174:175], v[232:233]\n\t"
      "v_pk_mul_f32 v[234:235], v[182:183], v[234:235]\n\t"
      "s_add_u32 s25, s25, 4096\n\t"
      "s_add_u32 m0, s85, 36864\n\t"
      "s_nop 0\n\t"
      "buffer_load_dwordx4 v152, s[36:39], s25 offen offset:0 sc1 lds\n\t"
      "v_pk_fma_f32 v[232:233], v[182:183], v[236:237], v[232:233] neg_lo:[1,0,0] neg_hi:[1,0,0]\n\t"
      "v_pk_fma_f32 v[234:235], v[174:175], v[238:239], v[234:235]\n\t"
      "v_cvt_pk_bf16_f32 v242, v232, v233\n\t"
      "v_cvt_pk_bf16_f32 v246, v234, v235\n\t"
      "v_lshlrev_b32 v232, 16, v189\n\t"
      "v_and_b32 v233, s89, v189\n\t"
      "v_lshlrev_b32 v234, 16, v193\n\t"
      "v_and_b32 v235, s89, v193\n\t"
      "v_lshlrev_b32 v236, 16, v197\n\t"
      "v_and_b32 v237, s89, v197\n\t"
      "v_lshlrev_b32 v238, 16, v201\n\t"
      "v_and_b32 v239, s89, v201\n\t"
      "v_pk_mul_f32 v[232:233], v[176:177], v[232:233]\n\t"
      "v_pk_mul_f32 v[234:235], v[184:185], v[234:235]\n\t"
      "v_pk_fma_f32 v[232:233], v[184:185], v[236:237], v[232:233] neg_lo:[1,0,0] neg_hi:[1,0,0]\n\t"
      "v_pk_fma_f32 v[234:235], v[176:177], v[238:239], v[234:235]\n\t"
      "v_cvt_pk_bf16_f32 v243, v232, v233\n\t"
      "buffer_load_dwordx4 v152, s[36:39], s25 offen offset:1024 sc1 lds\n\t"
      "v_cvt_pk_bf16_f32 v247, v234, v235\n\t"
      "buffer_store_dwordx4 v[240:243], v157, s[36:39], s93 offen sc1\n\t"
      "buffer_store_dwordx4 v[244:247], v158, s[36:39], s93 offen sc1\n\t"
      "v_lshlrev_b32 v222, 16, v222\n\t"
      "v_lshlrev_b32 v223, 16, v223\n\t"
      "v_mul_f32_e64 v223, -v223, v222\n\t"
      "v_cvt_f32_i32 v206, v206\n\t"
      "v_cvt_f32_i32 v207, v207\n\t"
      "v_cvt_f32_i32 v208, v208\n\t"
      "v_cvt_f32_i32 v209, v209\n\t"
      "v_cvt_f32_i32 v210, v210\n\t"
      "v_cvt_f32_i32 v211, v211\n\t"
      "v_cvt_f32_i32 v212, v212\n\t"
      "v_cvt_f32_i32 v213, v213\n\t"
      "v_cvt_f32_i32 v214, v214\n\t"
      "v_cvt_f32_i32 v215, v215\n\t"
      "v_cvt_f32_i32 v216, v216\n\t"
      "v_cvt_f32_i32 v217, v217\n\t"
      "v_cvt_f32_i32 v218, v218\n\t"
      "v_cvt_f32_i32 v219, v219\n\t"
      "buffer_load_dwordx4 v152, s[36:39], s25 offen offset:2048 sc1 lds\n\t"
      "v_cvt_f32_i32 v220, v220\n\t"
      "v_cvt_f32_i32 v221, v221\n\t"
      "v_mov_b32_dpp v224, v222 row_newbcast:0 row_mask:0xf bank_mask:0xf\n\t"
      "v_mov_b32_dpp v226, v223 row_newbcast:0 row_mask:0xf bank_mask:0xf\n\t"
      "v_pk_fma_f32 v[206:207], v[206:207], v[224:225], v[226:227] op_sel_hi:[1,0,0]\n\t"
      "v_mov_b32_dpp v228, v222 row_newbcast:1 row_mask:0xf bank_mask:0xf\n\t"
      "v_mov_b32_dpp v230, v223 row_newbcast:1 row_mask:0xf bank_mask:0xf\n\t"
      "v_pk_fma_f32 v[208:209], v[208:209], v[228:229], v[230:231] op_sel_hi:[1,0,0]\n\t"
      "v_mov_b32_dpp v224, v222 row_newbcast:2 row_mask:0xf bank_mask:0xf\n\t"
      "v_mov_b32_dpp v226, v223 row_newbcast:2 row_mask:0xf bank_mask:0xf\n\t"
      "v_pk_fma_f32 v[210:211], v[210:211], v[224:225], v[226:227] op_sel_hi:[1,0,0]\n\t"
      "v_mov_b32_dpp v228, v222 row_newbcast:3 row_mask:0xf bank_mask:0xf\n\t"
      "v_mov_b32_dpp v230, v223 row_newbcast:3 row_mask:0xf bank_mask:0xf\n\t"
      "v_pk_fma_f32 v[212:213], v[212:213], v[228:229], v[230:231] op_sel_hi:[1,0,0]\n\t"
      "v_mov_b32_dpp v224, v222 row_newbcast:4 row_mask:0xf bank_mask:0xf\n\t"
      "v_mov_b32_dpp v226, v223 row_newbcast:4 row_mask:0xf bank_mask:0xf\n\t"
      "v_pk_fma_f32 v[214:215], v[214:215], v[224:225], v[226:227] op_sel_hi:[1,0,0]\n\t"
      "buffer_load_dwordx4 v152, s[36:39], s25 offen offset:3072 sc1 lds\n\t"
      "s_add_u32 s25, s25, 126976\n\t"
      "v_mov_b32_dpp v228, v222 row_newbcast:5 row_mask:0xf bank_mask:0xf\n\t"
      "v_mov_b32_dpp v230, v223 row_newbcast:5 row_mask:0xf bank_mask:0xf\n\t"
      "v_pk_fma_f32 v[216:217], v[216:217], v[228:229], v[230:231] op_sel_hi:[1,0,0]\n\t"
      "v_mov_b32_dpp v224, v222 row_newbcast:6 row_mask:0xf bank_mask:0xf\n\t"
      "v_mov_b32_dpp v226, v223 row_newbcast:6 row_mask:0xf bank_mask:0xf\n\t"
      "v_pk_fma_f32 v[218:219], v[218:219], v[224:225], v[226:227] op_sel_hi:[1,0,0]\n\t"
      "v_mov_b32_dpp v228, v222 row_newbcast:7 row_mask:0xf bank_mask:0xf\n\t"
      "v_mov_b32_dpp v230, v223 row_newbcast:7 row_mask:0xf bank_mask:0xf\n\t"
      "v_pk_fma_f32 v[220:221], v[220:221], v[228:229], v[230:231] op_sel_hi:[1,0,0]\n\t"
      "v_cvt_pk_bf16_f32 v232, v206, v208\n\t"
      "v_cvt_pk_bf16_f32 v233, v210, v212\n\t"
      "v_cvt_pk_bf16_f32 v234, v214, v216\n\t"
      "v_cvt_pk_bf16_f32 v235, v218, v220\n\t"
      "buffer_store_dwordx4 v[232:235], v161, s[36:39], s93 offen offset:0 sc1\n\t"
      "v_cvt_pk_bf16_f32 v236, v207, v209\n\t"
      "v_cvt_pk_bf16_f32 v237, v211, v213\n\t"
      "v_cvt_pk_bf16_f32 v238, v215, v217\n\t"
      "v_cvt_pk_bf16_f32 v239, v219, v221\n\t"
      "buffer_store_dwordx4 v[236:239], v161, s[36:39], s93 offen offset:1024 sc1\n\t"
      "s_add_u32 s93, s93, 131072\n\t"
      "s_waitcnt lgkmcnt(0)\n\t"
      "v_mfma_f32_16x16x32_bf16 v[120:123], a[192:195], a[160:163], 0\n\t"
      "v_mfma_f32_16x16x32_bf16 v[120:123], a[196:199], a[164:167], v[120:123]\n\t"
      "v_mfma_f32_16x16x32_bf16 v[120:123], a[200:203], a[168:171], v[120:123]\n\t"
      "v_mfma_f32_16x16x32_bf16 v[120:123], a[204:207], a[172:175], v[120:123]\n\t"
      "s_nop 7\n\t"
      "v_max3_f32 v224, v120, v121, v122\n\t"
      "v_max_f32 v224, v224, v123\n\t"
      "v_mov_b32 v225, v224\n\t"
      "s_nop 0\n\t"
      "s_nop 0\n\t"
      "v_permlane16_swap_b32 v224, v225\n\t"
      "v_max_f32 v224, v224, v225\n\t"
      "v_mov_b32 v225, v224\n\t"
      "s_nop 0\n\t"
      "s_nop 0\n\t"
      "v_permlane32_swap_b32 v224, v225\n\t"
      "v_max_f32 v224, v224, v225\n\t"
      "v_sub_f32 v120, 0xc2200000, v224\n\t"
      "v_sub_f32 v121, 0xc2200000, v224\n\t"
      "v_sub_f32 v122, 0xc2200000, v224\n\t"
      "v_sub_f32 v123, 0xc2200000, v224\n\t"
      "s_nop 0\n\t"
      "s_nop 0\n\t"
      "v_mfma_f32_16x16x32_bf16 v[64:67], a[192:195], a[160:163], v[120:123]\n\t"
      "buffer_load_dwordx4 v[170:173], v153, s[40:43], s90 offen offset:0\n\t"
      "v_exp_f32 v93, v93\n\t"
      "v_mfma_f32_16x16x32_bf16 v[64:67], a[196:199], a[164:167], v[64:67]\n\t"
      "v_cvt_pk_bf16_f32 v110, v92, v93\n\t"
      "v_mfma_f32_16x16x32_bf16 v[64:67], a[200:203], a[168:171], v[64:67]\n\t"
      "v_exp_f32 v94, v94\n\t"
      "v_mfma_f32_16x16x32_bf16 v[64:67], a[204:207], a[172:175], v[64:67]\n\t"
      "buffer_load_dwordx4 v[174:177], v153, s[40:43], s90 offen offset:16\n\t"
      "v_mfma_f32_16x16x32_bf16 v[68:71], a[220:223], a[172:175], v[120:123]\n\t"
      "v_exp_f32 v95, v95\n\t"
      "v_mfma_f32_16x16x32_bf16 v[68:71], a[216:219], a[168:171], v[68:71]\n\t"
      "v_cvt_pk_bf16_f32 v111, v94, v95\n\t"
      "v_mfma_f32_16x16x32_bf16 v[68:71], a[212:215], a[164:167], v[68:71]\n\t"
      "buffer_load_dwordx4 v[178:181], v153, s[40:43], s90 offen offset:256\n\t"
      "v_mfma_f32_16x16x32_bf16 v[68:71], a[208:211], a[160:163], v[68:71]\n\t"
      "v_mfma_f32_16x16x32_bf16 v[72:75], a[224:227], a[160:163], v[120:123]\n\t"
      "v_exp_f32 v64, v64\n\t"
      "v_mfma_f32_16x16x32_bf16 v[72:75], a[228:231], a[164:167], v[72:75]\n\t"
      "v_mfma_f32_16x16x32_bf16 v[72:75], a[232:235], a[168:171], v[72:75]\n\t"
      "buffer_load_dwordx4 v[182:185], v153, s[40:43], s90 offen offset:272\n\t"
      "v_exp_f32 v65, v65\n\t"
      "v_mfma_f32_16x16x32_bf16 v[72:75], a[236:239], a[172:175], v[72:75]\n\t"
      "v_cvt_pk_bf16_f32 v96, v64, v65\n\t"
      "v_mfma_f32_16x16x32_bf16 v[76:79], a[252:255], a[172:175], v[120:123]\n\t"
      "v_exp_f32 v66, v66\n\t"
      "v_mfma_f32_16x16x32_bf16 v[76:79], a[248:251], a[168:171], v[76:79]\n\t"
      "buffer_load_dwordx2 v[202:203], v155, s[52:55], s92 offen\n\t"
      "v_mfma_f32_16x16x32_bf16 v[76:79], a[244:247], a[164:167], v[76:79]\n\t"
      "v_exp_f32 v67, v67\n\t"
      "v_mfma_f32_16x16x32_bf16 v[76:79], a[240:243], a[160:163], v[76:79]\n\t"
      "v_mfma_f32_16x16x32_bf16 a[4:7], v[104:107], v[0:3], 0\n\t"
      "v_exp_f32 v68, v68\n\t"
      "v_mfma_f32_16x16x32_bf16 a[4:7], v[108:111], v[4:7], a[4:7]\n\t"
      "buffer_load_dwordx2 v[204:205], v155, s[56:59], s92 offen\n\t"
      "v_cvt_pk_bf16_f32 v97, v66, v67\n\t"
      "v_mfma_f32_16x16x32_bf16 a[20:23], v[108:111], v[12:15], 0\n\t"
      "v_exp_f32 v69, v69\n\t"
      "v_mfma_f32_16x16x32_bf16 a[20:23], v[104:107], v[8:11], a[20:23]\n\t"
      "v_cvt_pk_bf16_f32 v98, v68, v69\n\t"
      "v_mfma_f32_16x16x32_bf16 a[36:39], v[104:107], v[16:19], 0\n\t"
      "buffer_load_dwordx4 v[186:189], v154, s[44:47], s91 offen offset:0\n\t"
      "v_exp_f32 v70, v70\n\t"
      "v_mfma_f32_16x16x32_bf16 a[36:39], v[108:111], v[20:23], a[36:39]\n\t"
      "v_mfma_f32_16x16x32_bf16 a[52:55], v[108:111], v[28:31], 0\n\t"
      "v_exp_f32 v71, v71\n\t"
      "v_mfma_f32_16x16x32_bf16 a[52:55], v[104:107], v[24:27], a[52:55]\n\t"
      "buffer_load_dwordx4 v[190:193], v154, s[44:47], s91 offen offset:128\n\t"
      "v_mfma_f32_16x16x32_bf16 a[68:71], v[104:107], v[32:35], 0\n\t"
      "v_exp_f32 v72, v72\n\t"
      "v_mfma_f32_16x16x32_bf16 a[68:71], v[108:111], v[36:39], a[68:71]\n\t"
      "v_cvt_pk_bf16_f32 v99, v70, v71\n\t"
      "v_mfma_f32_16x16x32_bf16 a[84:87], v[108:111], v[44:47], 0\n\t"
      "v_exp_f32 v73, v73\n\t"
      "v_mfma_f32_16x16x32_bf16 a[84:87], v[104:107], v[40:43], a[84:87]\n\t"
      "buffer_load_dwordx4 v[194:197], v154, s[48:51], s91 offen offset:0\n\t"
      "v_cvt_pk_bf16_f32 v100, v72, v73\n\t"
      "v_mfma_f32_16x16x32_bf16 a[100:103], v[104:107], v[48:51], 0\n\t"
      "v_exp_f32 v74, v74\n\t"
      "v_mfma_f32_16x16x32_bf16 a[100:103], v[108:111], v[52:55], a[100:103]\n\t"
      "v_mfma_f32_16x16x32_bf16 a[116:119], v[108:111], v[60:63], 0\n\t"
      "buffer_load_dwordx4 v[198:201], v154, s[48:51], s91 offen offset:128\n\t"
      "v_exp_f32 v75, v75\n\t"
      "v_mfma_f32_16x16x32_bf16 a[116:119], v[104:107], v[56:59], a[116:119]\n\t"
      "v_mfma_f32_16x16x32_bf16 v[132:135], v[104:107], v[248:251], 0\n\t"
      "v_exp_f32 v76, v76\n\t"
      "v_mfma_f32_16x16x32_bf16 v[132:135], v[108:111], v[248:251], v[132:135]\n\t"
      "v_cvt_pk_bf16_f32 v101, v74, v75\n\t"
      "s_waitcnt vmcnt(28)\n\t"
      "v_accvgpr_read_b32 v224, a176\n\t"
      "v_accvgpr_read_b32 v225, a180\n\t"
      "v_accvgpr_read_b32 v226, a12\n\t"
      "v_accvgpr_read_b32 v227, a28\n\t"
      "v_accvgpr_read_b32 v228, a44\n\t"
      "v_accvgpr_read_b32 v229, a60\n\t"
      "v_perm_b32 v230, v225, v224, s31\n\t"
      "v_perm_b32 v231, v228, v226, s31\n\t"
      "v_perm_b32 v232, v227, v229, s31\n\t"
      "v_perm_b32 v233, v225, v224, s84\n\t"
      "v_perm_b32 v234, v228, v226, s84\n\t"
      "v_perm_b32 v235, v227, v229, s84\n\t"
      "v_dot2_f32_bf16 v236, v230, v231, 0 neg_hi:[1,0,0]\n\t"
      "v_dot2_f32_bf16 v238, v230, v232, 0\n\t"
      "v_dot2_f32_bf16 v237, v233, v234, 0 neg_hi:[1,0,0]\n\t"
      "v_dot2_f32_bf16 v239, v233, v235, 0\n\t"
      "v_accvgpr_read_b32 v224, a177\n\t"
      "v_accvgpr_read_b32 v225, a181\n\t"
      "v_accvgpr_read_b32 v226, a13\n\t"
      "v_accvgpr_read_b32 v227, a29\n\t"
      "v_accvgpr_read_b32 v228, a45\n\t"
      "v_accvgpr_read_b32 v229, a61\n\t"
      "v_cvt_pk_bf16_f32 v240, v236, v237\n\t"
      "v_cvt_pk_bf16_f32 v244, v238, v239\n\t"
      "v_perm_b32 v230, v225, v224, s31\n\t"
      "v_perm_b32 v231, v228, v226, s31\n\t"
      "v_perm_b32 v232, v227, v229, s31\n\t"
      "v_perm_b32 v233, v225, v224, s84\n\t"
      "v_perm_b32 v234, v228, v226, s84\n\t"
      "v_perm_b32 v235, v227, v229, s84\n\t"
      "v_dot2_f32_bf16 v236, v230, v231, 0 neg_hi:[1,0,0]\n\t"
      "v_dot2_f32_bf16 v238, v230, v232, 0\n\t"
      "v_dot2_f32_bf16 v237, v233, v234, 0 neg_hi:[1,0,0]\n\t"
      "v_dot2_f32_bf16 v239, v233, v235, 0\n\t"
      "v_accvgpr_read_b32 v224, a178\n\t"
      "v_accvgpr_read_b32 v225, a182\n\t"
      "v_accvgpr_read_b32 v226, a14\n\t"
      "v_accvgpr_read_b32 v227, a30\n\t"
      "v_accvgpr_read_b32 v228, a46\n\t"
      "v_accvgpr_read_b32 v229, a62\n\t"
      "v_cvt_pk_bf16_f32 v241, v236, v237\n\t"
      "v_cvt_pk_bf16_f32 v245, v238, v239\n\t"
      "v_perm_b32 v230, v225, v224, s31\n\t"
      "v_perm_b32 v231, v228, v226, s31\n\t"
      "v_perm_b32 v232, v227, v229, s31\n\t"
      "v_perm_b32 v233, v225, v224, s84\n\t"
      "v_perm_b32 v234, v228, v226, s84\n\t"
      "v_perm_b32 v235, v227, v229, s84\n\t"
      "v_dot2_f32_bf16 v236, v230, v231, 0 neg_hi:[1,0,0]\n\t"
      "v_dot2_f32_bf16 v238, v230, v232, 0\n\t"
      "v_dot2_f32_bf16 v237, v233, v234, 0 neg_hi:[1,0,0]\n\t"
      "v_dot2_f32_bf16 v239, v233, v235, 0\n\t"
      "v_accvgpr_read_b32 v224, a179\n\t"
      "v_accvgpr_read_b32 v225, a183\n\t"
      "v_accvgpr_read_b32 v226, a15\n\t"
      "v_accvgpr_read_b32 v227, a31\n\t"
      "v_accvgpr_read_b32 v228, a47\n\t"
      "v_accvgpr_read_b32 v229, a63\n\t"
      "v_cvt_pk_bf16_f32 v242, v236, v237\n\t"
      "v_cvt_pk_bf16_f32 v246, v238, v239\n\t"
      "v_perm_b32 v230, v225, v224, s31\n\t"
      "v_perm_b32 v231, v228, v226, s31\n\t"
      "v_perm_b32 v232, v227, v229, s31\n\t"
      "v_perm_b32 v233, v225, v224, s84\n\t"
      "v_perm_b32 v234, v228, v226, s84\n\t"
      "v_perm_b32 v235, v227, v229, s84\n\t"
      "v_dot2_f32_bf16 v236, v230, v231, 0 neg_hi:[1,0,0]\n\t"
      "v_dot2_f32_bf16 v238, v230, v232, 0\n\t"
      "v_dot2_f32_bf16 v237, v233, v234, 0 neg_hi:[1,0,0]\n\t"
      "v_dot2_f32_bf16 v239, v233, v235, 0\n\t"
      "s_nop 1\n\t"
      "v_cvt_pk_bf16_f32 v243, v236, v237\n\t"
      "v_cvt_pk_bf16_f32 v247, v238, v239\n\t"
      "ds_write_b128 v252, v[240:243] offset:0\n\t"
      "ds_write_b128 v253, v[244:247] offset:0\n\t"
      "s_waitcnt vmcnt(10)\n\t"
      "buffer_store_dwordx2 v[168:169], v163, s[36:39], s94 offen sc1\n\t"
      "s_waitcnt vmcnt(23)\n\t"
      "v_accvgpr_read_b32 v224, a184\n\t"
      "v_accvgpr_read_b32 v225, a188\n\t"
      "v_accvgpr_read_b32 v226, a76\n\t"
      "v_accvgpr_read_b32 v227, a92\n\t"
      "v_accvgpr_read_b32 v228, a108\n\t"
      "v_accvgpr_read_b32 v229, a124\n\t"
      "v_perm_b32 v230, v225, v224, s31\n\t"
      "v_perm_b32 v231, v228, v226, s31\n\t"
      "v_perm_b32 v232, v227, v229, s31\n\t"
      "v_perm_b32 v233, v225, v224, s84\n\t"
      "v_perm_b32 v234, v228, v226, s84\n\t"
      "v_perm_b32 v235, v227, v229, s84\n\t"
      "v_dot2_f32_bf16 v236, v230, v231, 0 neg_hi:[1,0,0]\n\t"
      "v_dot2_f32_bf16 v238, v230, v232, 0\n\t"
      "v_dot2_f32_bf16 v237, v233, v234, 0 neg_hi:[1,0,0]\n\t"
      "v_dot2_f32_bf16 v239, v233, v235, 0\n\t"
      "v_accvgpr_read_b32 v224, a185\n\t"
      "v_accvgpr_read_b32 v225, a189\n\t"
      "v_accvgpr_read_b32 v226, a77\n\t"
      "v_accvgpr_read_b32 v227, a93\n\t"
      "v_accvgpr_read_b32 v228, a109\n\t"
      "v_accvgpr_read_b32 v229, a125\n\t"
      "v_cvt_pk_bf16_f32 v240, v236, v237\n\t"
      "v_cvt_pk_bf16_f32 v244, v238, v239\n\t"
      "v_perm_b32 v230, v225, v224, s31\n\t"
      "v_perm_b32 v231, v228, v226, s31\n\t"
      "v_perm_b32 v232, v227, v229, s31\n\t"
      "v_perm_b32 v233, v225, v224, s84\n\t"
      "v_perm_b32 v234, v228, v226, s84\n\t"
      "v_perm_b32 v235, v227, v229, s84\n\t"
      "v_dot2_f32_bf16 v236, v230, v231, 0 neg_hi:[1,0,0]\n\t"
      "v_dot2_f32_bf16 v238, v230, v232, 0\n\t"
      "v_dot2_f32_bf16 v237, v233, v234, 0 neg_hi:[1,0,0]\n\t"
      "v_dot2_f32_bf16 v239, v233, v235, 0\n\t"
      "v_accvgpr_read_b32 v224, a186\n\t"
      "v_accvgpr_read_b32 v225, a190\n\t"
      "v_accvgpr_read_b32 v226, a78\n\t"
      "v_accvgpr_read_b32 v227, a94\n\t"
      "v_accvgpr_read_b32 v228, a110\n\t"
      "v_accvgpr_read_b32 v229, a126\n\t"
      "v_cvt_pk_bf16_f32 v241, v236, v237\n\t"
      "v_cvt_pk_bf16_f32 v245, v238, v239\n\t"
      "v_perm_b32 v230, v225, v224, s31\n\t"
      "v_perm_b32 v231, v228, v226, s31\n\t"
      "v_perm_b32 v232, v227, v229, s31\n\t"
      "v_perm_b32 v233, v225, v224, s84\n\t"
      "v_perm_b32 v234, v228, v226, s84\n\t"
      "v_perm_b32 v235, v227, v229, s84\n\t"
      "v_dot2_f32_bf16 v236, v230, v231, 0 neg_hi:[1,0,0]\n\t"
      "v_dot2_f32_bf16 v238, v230, v232, 0\n\t"
      "v_dot2_f32_bf16 v237, v233, v234, 0 neg_hi:[1,0,0]\n\t"
      "v_dot2_f32_bf16 v239, v233, v235, 0\n\t"
      "v_accvgpr_read_b32 v224, a187\n\t"
      "v_accvgpr_read_b32 v225, a191\n\t"
      "v_accvgpr_read_b32 v226, a79\n\t"
      "v_accvgpr_read_b32 v227, a95\n\t"
      "v_accvgpr_read_b32 v228, a111\n\t"
      "v_accvgpr_read_b32 v229, a127\n\t"
      "v_cvt_pk_bf16_f32 v242, v236, v237\n\t"
      "v_cvt_pk_bf16_f32 v246, v238, v239\n\t"
      "v_perm_b32 v230, v225, v224, s31\n\t"
      "v_perm_b32 v231, v228, v226, s31\n\t"
      "v_perm_b32 v232, v227, v229, s31\n\t"
      "v_perm_b32 v233, v225, v224, s84\n\t"
      "v_perm_b32 v234, v228, v226, s84\n\t"
      "v_perm_b32 v235, v227, v229, s84\n\t"
      "v_dot2_f32_bf16 v236, v230, v231, 0 neg_hi:[1,0,0]\n\t"
      "v_dot2_f32_bf16 v238, v230, v232, 0\n\t"
      "v_dot2_f32_bf16 v237, v233, v234, 0 neg_hi:[1,0,0]\n\t"
      "v_dot2_f32_bf16 v239, v233, v235, 0\n\t"
      "s_nop 1\n\t"
      "v_cvt_pk_bf16_f32 v243, v236, v237\n\t"
      "v_cvt_pk_bf16_f32 v247, v238, v239\n\t"
      "ds_write_b128 v253, v[240:243] offset:2048\n\t"
      "ds_write_b128 v252, v[244:247] offset:2048\n\t"
      "ds_read_b128 a[176:179], v148\n\t"
      "ds_read_b128 a[180:183], v149\n\t"
      "ds_read_b128 a[184:187], v150\n\t"
      "ds_read_b128 a[188:191], v151\n\t"
      "s_waitcnt lgkmcnt(0)\n\t"
      "v_mfma_f32_16x16x32_bf16 v[124:127], a[192:195], a[176:179], 0\n\t"
      "v_mfma_f32_16x16x32_bf16 v[124:127], a[196:199], a[180:183], v[124:127]\n\t"
      "v_mfma_f32_16x16x32_bf16 v[124:127], a[200:203], a[184:187], v[124:127]\n\t"
      "v_mfma_f32_16x16x32_bf16 v[124:127], a[204:207], a[188:191], v[124:127]\n\t"
      "v_mov_b32 v148, 0\n\t"
      "v_mov_b32 v149, 0\n\t"
      "v_mov_b32 v150, 0\n\t"
      "v_mov_b32 v151, 0\n\t"
      "v_accvgpr_write_b32 a12, 0\n\t"
      "v_accvgpr_write_b32 a13, 0\n\t"
      "v_accvgpr_write_b32 a14, 0\n\t"
      "v_accvgpr_write_b32 a15, 0\n\t"
      "v_max3_f32 v224, v124, v125, v126\n\t"
      "v_max_f32 v224, v224, v127\n\t"
      "v_mov_b32 v225, v224\n\t"
      "s_nop 0\n\t"
      "s_nop 0\n\t"
      "v_permlane16_swap_b32 v224, v225\n\t"
      "v_max_f32 v224, v224, v225\n\t"
      "v_mov_b32 v225, v224\n\t"
      "s_nop 0\n\t"
      "s_nop 0\n\t"
      "v_permlane32_swap_b32 v224, v225\n\t"
      "v_max_f32 v224, v224, v225\n\t"
      "v_sub_f32 v124, 0xc2200000, v224\n\t"
      "v_sub_f32 v125, 0xc2200000, v224\n\t"
      "v_sub_f32 v126, 0xc2200000, v224\n\t"
      "v_sub_f32 v127, 0xc2200000, v224\n\t"
      "s_waitcnt vmcnt(13)\n\t"
      "s_barrier\n\t"
      "v_mfma_f32_16x16x32_bf16 v[80:83], a[192:195], a[176:179], v[124:127]\n\t"
      "s_add_u32 m0, s85, 65536\n\t"
      "s_nop 0\n\t"
      "buffer_load_dwordx4 v152, s[36:39], s26 offen offset:0 sc1 lds\n\t"
      "v_exp_f32 v77, v77\n\t"
      "v_accvgpr_write_b32 a127, 0\n\t"
      "v_mfma_f32_16x16x32_bf16 v[80:83], a[196:199], a[180:183], v[80:83]\n\t"
      "ds_read_b128 a[192:195], v136 offset:32768\n\t"
      "v_cvt_pk_bf16_f32 v102, v76, v77\n\t"
      "v_accvgpr_write_b32 a126, 0\n\t"
      "v_mfma_f32_16x16x32_bf16 v[80:83], a[200:203], a[184:187], v[80:83]\n\t"
      "v_exp_f32 v78, v78\n\t"
      "v_accvgpr_write_b32 a125, 0\n\t"
      "v_mfma_f32_16x16x32_bf16 v[80:83], a[204:207], a[188:191], v[80:83]\n\t"
      "ds_read_b128 a[196:199], v137 offset:32768\n\t"
      "v_accvgpr_write_b32 a124, 0\n\t"
      "v_mfma_f32_16x16x32_bf16 v[84:87], a[220:223], a[188:191], v[124:127]\n\t"
      "buffer_load_dwordx4 v152, s[36:39], s26 offen offset:1024 sc1 lds\n\t"
      "v_exp_f32 v79, v79\n\t"
      "v_accvgpr_write_b32 a111, 0\n\t"
      "v_mfma_f32_16x16x32_bf16 v[84:87], a[216:219], a[184:187], v[84:87]\n\t"
      "ds_read_b128 a[200:203], v138 offset:32768\n\t"
      "v_cvt_pk_bf16_f32 v103, v78, v79\n\t"
      "v_accvgpr_write_b32 a110, 0\n\t"
      "v_mfma_f32_16x16x32_bf16 v[84:87], a[212:215], a[180:183], v[84:87]\n\t"
      "v_accvgpr_write_b32 a109, 0\n\t"
      "v_mfma_f32_16x16x32_bf16 v[84:87], a[208:211], a[176:179], v[84:87]\n\t"
      "ds_read_b128 a[204:207], v139 offset:32768\n\t"
      "v_accvgpr_write_b32 a108, 0\n\t"
      "v_mfma_f32_16x16x32_bf16 v[88:91], a[224:227], a[176:179], v[124:127]\n\t"
      "buffer_load_dwordx4 v152, s[36:39], s26 offen offset:2048 sc1 lds\n\t"
      "v_exp_f32 v80, v80\n\t"
      "v_accvgpr_write_b32 a95, 0\n\t"
      "v_mfma_f32_16x16x32_bf16 v[88:91], a[228:231], a[180:183], v[88:91]\n\t"
      "ds_read_b128 a[208:211], v136 offset:36864\n\t"
      "v_accvgpr_write_b32 a94, 0\n\t"
      "v_mfma_f32_16x16x32_bf16 v[88:91], a[232:235], a[184:187], v[88:91]\n\t"
      "v_exp_f32 v81, v81\n\t"
      "v_accvgpr_write_b32 a93, 0\n\t"
      "v_mfma_f32_16x16x32_bf16 v[88:91], a[236:239], a[188:191], v[88:91]\n\t"
      "ds_read_b128 a[212:215], v137 offset:36864\n\t"
      "v_cvt_pk_bf16_f32 v104, v80, v81\n\t"
      "v_accvgpr_write_b32 a92, 0\n\t"
      "v_mfma_f32_16x16x32_bf16 v[92:95], a[252:255], a[188:191], v[124:127]\n\t"
      "buffer_load_dwordx4 v152, s[36:39], s26 offen offset:3072 sc1 lds\n\t"
      "v_exp_f32 v82, v82\n\t"
      "v_accvgpr_write_b32 a79, 0\n\t"
      "v_mfma_f32_16x16x32_bf16 v[92:95], a[248:251], a[184:187], v[92:95]\n\t"
      "ds_read_b128 a[216:219], v138 offset:36864\n\t"
      "v_accvgpr_write_b32 a78, 0\n\t"
      "v_mfma_f32_16x16x32_bf16 v[92:95], a[244:247], a[180:183], v[92:95]\n\t"
      "v_exp_f32 v83, v83\n\t"
      "v_accvgpr_write_b32 a77, 0\n\t"
      "v_mfma_f32_16x16x32_bf16 v[92:95], a[240:243], a[176:179], v[92:95]\n\t"
      "ds_read_b128 a[220:223], v139 offset:36864\n\t"
      "v_cvt_pk_bf16_f32 v105, v82, v83\n\t"
      "v_accvgpr_write_b32 a76, 0\n\t"
      "v_mfma_f32_16x16x32_bf16 a[8:11], v[96:99], v[0:3], 0\n\t"
      "v_exp_f32 v84, v84\n\t"
      "v_accvgpr_write_b32 a63, 0\n\t"
      "v_mfma_f32_16x16x32_bf16 a[8:11], v[100:103], v[4:7], a[8:11]\n\t"
      "s_add_u32 s26, s26, 4096\n\t"
      "s_add_u32 m0, s85, 69632\n\t"
      "s_nop 0\n\t"
      "buffer_load_dwordx4 v152, s[36:39], s26 offen offset:0 sc1 lds\n\t"
      "ds_read_b128 a[224:227], v136 offset:40960\n\t"
      "v_accvgpr_write_b32 a62, 0\n\t"
      "v_mfma_f32_16x16x32_bf16 a[24:27], v[100:103], v[12:15], 0\n\t"
      "v_exp_f32 v85, v85\n\t"
      "v_accvgpr_write_b32 a61, 0\n\t"
      "v_mfma_f32_16x16x32_bf16 a[24:27], v[96:99], v[8:11], a[24:27]\n\t"
      "ds_read_b128 a[228:231], v137 offset:40960\n\t"
      "v_cvt_pk_bf16_f32 v106, v84, v85\n\t"
      "v_accvgpr_write_b32 a60, 0\n\t"
      "v_mfma_f32_16x16x32_bf16 a[40:43], v[96:99], v[16:19], 0\n\t"
      "v_exp_f32 v86, v86\n\t"
      "v_accvgpr_write_b32 a47, 0\n\t"
      "v_mfma_f32_16x16x32_bf16 a[40:43], v[100:103], v[20:23], a[40:43]\n\t"
      "buffer_load_dwordx4 v152, s[36:39], s26 offen offset:1024 sc1 lds\n\t"
      "ds_read_b128 a[232:235], v138 offset:40960\n\t"
      "v_accvgpr_write_b32 a46, 0\n\t"
      "v_mfma_f32_16x16x32_bf16 a[56:59], v[100:103], v[28:31], 0\n\t"
      "v_exp_f32 v87, v87\n\t"
      "v_accvgpr_write_b32 a45, 0\n\t"
      "v_mfma_f32_16x16x32_bf16 a[56:59], v[96:99], v[24:27], a[56:59]\n\t"
      "ds_read_b128 a[236:239], v139 offset:40960\n\t"
      "v_cvt_pk_bf16_f32 v107, v86, v87\n\t"
      "v_accvgpr_write_b32 a44, 0\n\t"
      "v_mfma_f32_16x16x32_bf16 a[72:75], v[96:99], v[32:35], 0\n\t"
      "v_exp_f32 v88, v88\n\t"
      "v_accvgpr_write_b32 a31, 0\n\t"
      "v_mfma_f32_16x16x32_bf16 a[72:75], v[100:103], v[36:39], a[72:75]\n\t"
      "buffer_load_dwordx4 v152, s[36:39], s26 offen offset:2048 sc1 lds\n\t"
      "ds_read_b128 a[240:243], v136 offset:45056\n\t"
      "v_accvgpr_write_b32 a30, 0\n\t"
      "v_mfma_f32_16x16x32_bf16 a[88:91], v[100:103], v[44:47], 0\n\t"
      "v_exp_f32 v89, v89\n\t"
      "v_accvgpr_write_b32 a29, 0\n\t"
      "v_mfma_f32_16x16x32_bf16 a[88:91], v[96:99], v[40:43], a[88:91]\n\t"
      "ds_read_b128 a[244:247], v137 offset:45056\n\t"
      "v_cvt_pk_bf16_f32 v108, v88, v89\n\t"
      "v_accvgpr_write_b32 a28, 0\n\t"
      "v_mfma_f32_16x16x32_bf16 a[104:107], v[96:99], v[48:51], 0\n\t"
      "v_exp_f32 v90, v90\n\t"
      "v_mfma_f32_16x16x32_bf16 a[104:107], v[100:103], v[52:55], a[104:107]\n\t"
      "buffer_load_dwordx4 v152, s[36:39], s26 offen offset:3072 sc1 lds\n\t"
      "ds_read_b128 a[248:251], v138 offset:45056\n\t"
      "v_mfma_f32_16x16x32_bf16 a[120:123], v[100:103], v[60:63], 0\n\t"
      "v_exp_f32 v91, v91\n\t"
      "v_mfma_f32_16x16x32_bf16 a[120:123], v[96:99], v[56:59], a[120:123]\n\t"
      "ds_read_b128 a[252:255], v139 offset:45056\n\t"
      "v_cvt_pk_bf16_f32 v109, v90, v91\n\t"
      "v_mfma_f32_16x16x32_bf16 v[140:143], v[96:99], v[248:251], 0\n\t"
      "s_waitcnt lgkmcnt(12)\n\t"
      "v_exp_f32 v92, v92\n\t"
      "v_mfma_f32_16x16x32_bf16 v[140:143], v[100:103], v[248:251], v[140:143]\n\t"
      "s_branch .Lattn_u1\n\t"
      ".p2align 6\n\t"
      ".Lattn_loop:\n\t"
      "v_mfma_f32_16x16x32_bf16 v[64:67], a[192:195], a[128:131], v[112:115]\n\t"
      "s_add_u32 s25, s25, 126976\n\t"
      "v_exp_f32 v93, v93\n\t"
      "v_mfma_f32_16x16x32_bf16 v[64:67], a[196:199], a[132:135], v[64:67]\n\t"
      "v_cvt_pk_bf16_f32 v110, v92, v93\n\t"
      "v_mfma_f32_16x16x32_bf16 v[64:67], a[200:203], a[136:139], v[64:67]\n\t"
      "s_waitcnt lgkmcnt(8)\n\t"
      "v_exp_f32 v94, v94\n\t"
      "v_mfma_f32_16x16x32_bf16 v[64:67], a[204:207], a[140:143], v[64:67]\n\t"
      "buffer_load_dwordx2 v[202:203], v155, s[52:55], s92 offen\n\t"
      "v_mfma_f32_16x16x32_bf16 v[68:71], a[220:223], a[140:143], v[112:115]\n\t"
      "v_exp_f32 v95, v95\n\t"
      "v_mfma_f32_16x16x32_bf16 v[68:71], a[216:219], a[136:139], v[68:71]\n\t"
      "s_waitcnt lgkmcnt(4)\n\t"
      "v_cvt_pk_bf16_f32 v111, v94, v95\n\t"
      "v_mfma_f32_16x16x32_bf16 v[68:71], a[212:215], a[132:135], v[68:71]\n\t"
      "buffer_load_dwordx2 v[204:205], v155, s[56:59], s92 offen\n\t"
      "v_mfma_f32_16x16x32_bf16 v[68:71], a[208:211], a[128:131], v[68:71]\n\t"
      "v_mfma_f32_16x16x32_bf16 v[72:75], a[224:227], a[128:131], v[112:115]\n\t"
      "v_exp_f32 v64, v64\n\t"
      "v_mfma_f32_16x16x32_bf16 v[72:75], a[228:231], a[132:135], v[72:75]\n\t"
      "v_mfma_f32_16x16x32_bf16 v[72:75], a[232:235], a[136:139], v[72:75]\n\t"
      "s_waitcnt lgkmcnt(0)\n\t"
      "v_exp_f32 v65, v65\n\t"
      "v_mfma_f32_16x16x32_bf16 v[72:75], a[236:239], a[140:143], v[72:75]\n\t"
      "v_cvt_pk_bf16_f32 v96, v64, v65\n\t"
      "v_mfma_f32_16x16x32_bf16 v[76:79], a[252:255], a[140:143], v[112:115]\n\t"
      "v_exp_f32 v66, v66\n\t"
      "v_mfma_f32_16x16x32_bf16 v[76:79], a[248:251], a[136:139], v[76:79]\n\t"
      "buffer_load_dwordx4 v[186:189], v154, s[44:47], s91 offen offset:0\n\t"
      "v_mfma_f32_16x16x32_bf16 v[76:79], a[244:247], a[132:135], v[76:79]\n\t"
      "v_exp_f32 v67, v67\n\t"
      "v_mfma_f32_16x16x32_bf16 v[76:79], a[240:243], a[128:131], v[76:79]\n\t"
      "v_cvt_pk_bf16_f32 v97, v66, v67\n\t"
      "v_mfma_f32_16x16x32_bf16 a[12:15], v[104:107], v[0:3], a[12:15]\n\t"
      "v_exp_f32 v68, v68\n\t"
      "v_mfma_f32_16x16x32_bf16 a[12:15], v[108:111], v[4:7], a[12:15]\n\t"
      "ds_read_b128 v[0:3], v144 offset:16384\n\t"
      "v_mfma_f32_16x16x32_bf16 a[28:31], v[108:111], v[12:15], a[28:31]\n\t"
      "v_exp_f32 v69, v69\n\t"
      "v_mfma_f32_16x16x32_bf16 a[28:31], v[104:107], v[8:11], a[28:31]\n\t"
      "ds_read_b128 v[4:7], v144 offset:24576\n\t"
      "v_cvt_pk_bf16_f32 v98, v68, v69\n\t"
      "v_mfma_f32_16x16x32_bf16 a[44:47], v[104:107], v[16:19], a[44:47]\n\t"
      "v_exp_f32 v70, v70\n\t"
      "v_mfma_f32_16x16x32_bf16 a[44:47], v[108:111], v[20:23], a[44:47]\n\t"
      "ds_read_b128 v[8:11], v144 offset:17408\n\t"
      "v_mfma_f32_16x16x32_bf16 a[60:63], v[108:111], v[28:31], a[60:63]\n\t"
      "buffer_load_dwordx4 v[190:193], v154, s[44:47], s91 offen offset:128\n\t"
      "v_exp_f32 v71, v71\n\t"
      "v_mfma_f32_16x16x32_bf16 a[60:63], v[104:107], v[24:27], a[60:63]\n\t"
      "ds_read_b128 v[12:15], v144 offset:25600\n\t"
      "v_cvt_pk_bf16_f32 v99, v70, v71\n\t"
      "v_mfma_f32_16x16x32_bf16 a[76:79], v[104:107], v[32:35], a[76:79]\n\t"
      "v_exp_f32 v72, v72\n\t"
      "v_mfma_f32_16x16x32_bf16 a[76:79], v[108:111], v[36:39], a[76:79]\n\t"
      "ds_read_b128 v[16:19], v145 offset:16384\n\t"
      "v_mfma_f32_16x16x32_bf16 a[92:95], v[108:111], v[44:47], a[92:95]\n\t"
      "v_exp_f32 v73, v73\n\t"
      "v_mfma_f32_16x16x32_bf16 a[92:95], v[104:107], v[40:43], a[92:95]\n\t"
      "ds_read_b128 v[20:23], v145 offset:24576\n\t"
      "v_cvt_pk_bf16_f32 v100, v72, v73\n\t"
      "v_mfma_f32_16x16x32_bf16 a[108:111], v[104:107], v[48:51], a[108:111]\n\t"
      "v_exp_f32 v74, v74\n\t"
      "v_mfma_f32_16x16x32_bf16 a[108:111], v[108:111], v[52:55], a[108:111]\n\t"
      "ds_read_b128 v[24:27], v145 offset:17408\n\t"
      "v_mfma_f32_16x16x32_bf16 a[124:127], v[108:111], v[60:63], a[124:127]\n\t"
      "v_exp_f32 v75, v75\n\t"
      "v_mfma_f32_16x16x32_bf16 a[124:127], v[104:107], v[56:59], a[124:127]\n\t"
      "ds_read_b128 v[28:31], v145 offset:25600\n\t"
      "v_cvt_pk_bf16_f32 v101, v74, v75\n\t"
      "v_mfma_f32_16x16x32_bf16 v[148:151], v[104:107], v[248:251], v[148:151]\n\t"
      "v_exp_f32 v76, v76\n\t"
      "v_mfma_f32_16x16x32_bf16 v[148:151], v[108:111], v[248:251], v[148:151]\n\t"
      "ds_read_b128 v[32:35], v146 offset:16384\n\t"
      "v_mfma_f32_16x16x32_bf16 v[80:83], a[192:195], a[144:147], v[116:119]\n\t"
      "v_exp_f32 v77, v77\n\t"
      "v_mfma_f32_16x16x32_bf16 v[80:83], a[196:199], a[148:151], v[80:83]\n\t"
      "ds_read_b128 v[36:39], v146 offset:24576\n\t"
      "v_cvt_pk_bf16_f32 v102, v76, v77\n\t"
      "v_mfma_f32_16x16x32_bf16 v[80:83], a[200:203], a[152:155], v[80:83]\n\t"
      "v_exp_f32 v78, v78\n\t"
      "v_mfma_f32_16x16x32_bf16 v[80:83], a[204:207], a[156:159], v[80:83]\n\t"
      "ds_read_b128 v[40:43], v146 offset:17408\n\t"
      "v_mfma_f32_16x16x32_bf16 v[84:87], a[220:223], a[156:159], v[116:119]\n\t"
      "v_exp_f32 v79, v79\n\t"
      "v_mfma_f32_16x16x32_bf16 v[84:87], a[216:219], a[152:155], v[84:87]\n\t"
      "ds_read_b128 v[44:47], v146 offset:25600\n\t"
      "v_cvt_pk_bf16_f32 v103, v78, v79\n\t"
      "v_mfma_f32_16x16x32_bf16 v[84:87], a[212:215], a[148:151], v[84:87]\n\t"
      "buffer_load_dwordx4 v[194:197], v154, s[48:51], s91 offen offset:0\n\t"
      "v_mfma_f32_16x16x32_bf16 v[84:87], a[208:211], a[144:147], v[84:87]\n\t"
      "ds_read_b128 v[48:51], v147 offset:16384\n\t"
      "v_mfma_f32_16x16x32_bf16 v[88:91], a[224:227], a[144:147], v[116:119]\n\t"
      "v_exp_f32 v80, v80\n\t"
      "v_mfma_f32_16x16x32_bf16 v[88:91], a[228:231], a[148:151], v[88:91]\n\t"
      "ds_read_b128 v[52:55], v147 offset:24576\n\t"
      "v_mfma_f32_16x16x32_bf16 v[88:91], a[232:235], a[152:155], v[88:91]\n\t"
      "v_exp_f32 v81, v81\n\t"
      "v_mfma_f32_16x16x32_bf16 v[88:91], a[236:239], a[156:159], v[88:91]\n\t"
      "ds_read_b128 v[56:59], v147 offset:17408\n\t"
      "v_cvt_pk_bf16_f32 v104, v80, v81\n\t"
      "v_mfma_f32_16x16x32_bf16 v[92:95], a[252:255], a[156:159], v[116:119]\n\t"
      "v_exp_f32 v82, v82\n\t"
      "v_mfma_f32_16x16x32_bf16 v[92:95], a[248:251], a[152:155], v[92:95]\n\t"
      "ds_read_b128 v[60:63], v147 offset:25600\n\t"
      "v_mfma_f32_16x16x32_bf16 v[92:95], a[244:247], a[148:151], v[92:95]\n\t"
      "v_exp_f32 v83, v83\n\t"
      "v_mfma_f32_16x16x32_bf16 v[92:95], a[240:243], a[144:147], v[92:95]\n\t"
      "s_waitcnt lgkmcnt(8)\n\t"
      "v_cvt_pk_bf16_f32 v105, v82, v83\n\t"
      "v_mfma_f32_16x16x32_bf16 a[0:3], v[96:99], v[0:3], a[0:3]\n\t"
      "v_exp_f32 v84, v84\n\t"
      "v_mfma_f32_16x16x32_bf16 a[0:3], v[100:103], v[4:7], a[0:3]\n\t"
      "buffer_load_dwordx4 v[198:201], v154, s[48:51], s91 offen offset:128\n\t"
      "v_mfma_f32_16x16x32_bf16 a[16:19], v[100:103], v[12:15], a[16:19]\n\t"
      "v_exp_f32 v85, v85\n\t"
      "v_mfma_f32_16x16x32_bf16 a[16:19], v[96:99], v[8:11], a[16:19]\n\t"
      "v_cvt_pk_bf16_f32 v106, v84, v85\n\t"
      "v_mfma_f32_16x16x32_bf16 a[32:35], v[96:99], v[16:19], a[32:35]\n\t"
      "v_exp_f32 v86, v86\n\t"
      "v_mfma_f32_16x16x32_bf16 a[32:35], v[100:103], v[20:23], a[32:35]\n\t"
      "v_mfma_f32_16x16x32_bf16 a[48:51], v[100:103], v[28:31], a[48:51]\n\t"
      "s_waitcnt lgkmcnt(0)\n\t"
      "v_exp_f32 v87, v87\n\t"
      "v_mfma_f32_16x16x32_bf16 a[48:51], v[96:99], v[24:27], a[48:51]\n\t"
      "v_cvt_pk_bf16_f32 v107, v86, v87\n\t"
      "v_mfma_f32_16x16x32_bf16 a[64:67], v[96:99], v[32:35], a[64:67]\n\t"
      "s_add_u32 m0, s85, 65536\n\t"
      "v_exp_f32 v88, v88\n\t"
      "v_mfma_f32_16x16x32_bf16 a[64:67], v[100:103], v[36:39], a[64:67]\n\t"
      "buffer_load_dwordx4 v152, s[36:39], s26 offen offset:0 sc1 lds\n\t"
      "v_mfma_f32_16x16x32_bf16 a[80:83], v[100:103], v[44:47], a[80:83]\n\t"
      "v_exp_f32 v89, v89\n\t"
      "v_mfma_f32_16x16x32_bf16 a[80:83], v[96:99], v[40:43], a[80:83]\n\t"
      "v_cvt_pk_bf16_f32 v108, v88, v89\n\t"
      "v_mfma_f32_16x16x32_bf16 a[96:99], v[96:99], v[48:51], a[96:99]\n\t"
      "v_exp_f32 v90, v90\n\t"
      "v_mfma_f32_16x16x32_bf16 a[96:99], v[100:103], v[52:55], a[96:99]\n\t"
      "v_mfma_f32_16x16x32_bf16 a[112:115], v[100:103], v[60:63], a[112:115]\n\t"
      "v_exp_f32 v91, v91\n\t"
      "v_mfma_f32_16x16x32_bf16 a[112:115], v[96:99], v[56:59], a[112:115]\n\t"
      "v_cvt_pk_bf16_f32 v109, v90, v91\n\t"
      "v_mfma_f32_16x16x32_bf16 v[128:131], v[96:99], v[248:251], v[128:131]\n\t"
      "v_exp_f32 v92, v92\n\t"
      "v_mfma_f32_16x16x32_bf16 v[128:131], v[100:103], v[248:251], v[128:131]\n\t"
      "buffer_load_dwordx4 v152, s[36:39], s26 offen offset:1024 sc1 lds\n\t"
      "v_mfma_f32_16x16x32_bf16 v[64:67], a[192:195], a[160:163], v[120:123]\n\t"
      "v_exp_f32 v93, v93\n\t"
      "v_mfma_f32_16x16x32_bf16 v[64:67], a[196:199], a[164:167], v[64:67]\n\t"
      "v_cvt_pk_bf16_f32 v110, v92, v93\n\t"
      "v_mfma_f32_16x16x32_bf16 v[64:67], a[200:203], a[168:171], v[64:67]\n\t"
      "v_exp_f32 v94, v94\n\t"
      "v_mfma_f32_16x16x32_bf16 v[64:67], a[204:207], a[172:175], v[64:67]\n\t"
      "v_mfma_f32_16x16x32_bf16 v[68:71], a[220:223], a[172:175], v[120:123]\n\t"
      "v_exp_f32 v95, v95\n\t"
      "v_mfma_f32_16x16x32_bf16 v[68:71], a[216:219], a[168:171], v[68:71]\n\t"
      "v_cvt_pk_bf16_f32 v111, v94, v95\n\t"
      "v_mfma_f32_16x16x32_bf16 v[68:71], a[212:215], a[164:167], v[68:71]\n\t"
      "buffer_load_dwordx4 v152, s[36:39], s26 offen offset:2048 sc1 lds\n\t"
      "v_mfma_f32_16x16x32_bf16 v[68:71], a[208:211], a[160:163], v[68:71]\n\t"
      "v_mfma_f32_16x16x32_bf16 v[72:75], a[224:227], a[160:163], v[120:123]\n\t"
      "v_exp_f32 v64, v64\n\t"
      "v_mfma_f32_16x16x32_bf16 v[72:75], a[228:231], a[164:167], v[72:75]\n\t"
      "v_mfma_f32_16x16x32_bf16 v[72:75], a[232:235], a[168:171], v[72:75]\n\t"
      "v_exp_f32 v65, v65\n\t"
      "v_mfma_f32_16x16x32_bf16 v[72:75], a[236:239], a[172:175], v[72:75]\n\t"
      "v_cvt_pk_bf16_f32 v96, v64, v65\n\t"
      "v_mfma_f32_16x16x32_bf16 v[76:79], a[252:255], a[172:175], v[120:123]\n\t"
      "v_exp_f32 v66, v66\n\t"
      "v_mfma_f32_16x16x32_bf16 v[76:79], a[248:251], a[168:171], v[76:79]\n\t"
      "v_mfma_f32_16x16x32_bf16 v[76:79], a[244:247], a[164:167], v[76:79]\n\t"
      "v_exp_f32 v67, v67\n\t"
      "v_mfma_f32_16x16x32_bf16 v[76:79], a[240:243], a[160:163], v[76:79]\n\t"
      "buffer_load_dwordx4 v152, s[36:39], s26 offen offset:3072 sc1 lds\n\t"
      "v_mfma_f32_16x16x32_bf16 a[4:7], v[104:107], v[0:3], a[4:7]\n\t"
      "s_add_u32 s26, s26, 4096\n\t"
      "v_exp_f32 v68, v68\n\t"
      "v_mfma_f32_16x16x32_bf16 a[4:7], v[108:111], v[4:7], a[4:7]\n\t"
      "v_cvt_pk_bf16_f32 v97, v66, v67\n\t"
      "v_mfma_f32_16x16x32_bf16 a[20:23], v[108:111], v[12:15], a[20:23]\n\t"
      "s_add_u32 m0, s85, 69632\n\t"
      "v_exp_f32 v69, v69\n\t"
      "v_mfma_f32_16x16x32_bf16 a[20:23], v[104:107], v[8:11], a[20:23]\n\t"
      "v_cvt_pk_bf16_f32 v98, v68, v69\n\t"
      "v_mfma_f32_16x16x32_bf16 a[36:39], v[104:107], v[16:19], a[36:39]\n\t"
      "v_exp_f32 v70, v70\n\t"
      "v_mfma_f32_16x16x32_bf16 a[36:39], v[108:111], v[20:23], a[36:39]\n\t"
      "v_mfma_f32_16x16x32_bf16 a[52:55], v[108:111], v[28:31], a[52:55]\n\t"
      "v_exp_f32 v71, v71\n\t"
      "v_mfma_f32_16x16x32_bf16 a[52:55], v[104:107], v[24:27], a[52:55]\n\t"
      "buffer_load_dwordx4 v152, s[36:39], s26 offen offset:0 sc1 lds\n\t"
      "v_mfma_f32_16x16x32_bf16 a[68:71], v[104:107], v[32:35], a[68:71]\n\t"
      "v_exp_f32 v72, v72\n\t"
      "v_mfma_f32_16x16x32_bf16 a[68:71], v[108:111], v[36:39], a[68:71]\n\t"
      "v_cvt_pk_bf16_f32 v99, v70, v71\n\t"
      "v_mfma_f32_16x16x32_bf16 a[84:87], v[108:111], v[44:47], a[84:87]\n\t"
      "v_exp_f32 v73, v73\n\t"
      "v_mfma_f32_16x16x32_bf16 a[84:87], v[104:107], v[40:43], a[84:87]\n\t"
      "v_cvt_pk_bf16_f32 v100, v72, v73\n\t"
      "v_mfma_f32_16x16x32_bf16 a[100:103], v[104:107], v[48:51], a[100:103]\n\t"
      "s_waitcnt vmcnt(12)\n\t"
      "v_exp_f32 v74, v74\n\t"
      "v_mfma_f32_16x16x32_bf16 a[100:103], v[108:111], v[52:55], a[100:103]\n\t"
      "buffer_store_dwordx2 v[168:169], v163, s[36:39], s94 offen sc1\n\t"
      "v_mfma_f32_16x16x32_bf16 a[116:119], v[108:111], v[60:63], a[116:119]\n\t"
      "v_exp_f32 v75, v75\n\t"
      "v_mfma_f32_16x16x32_bf16 a[116:119], v[104:107], v[56:59], a[116:119]\n\t"
      "buffer_load_dwordx4 v152, s[36:39], s26 offen offset:1024 sc1 lds\n\t"
      "v_mfma_f32_16x16x32_bf16 v[132:135], v[104:107], v[248:251], v[132:135]\n\t"
      "s_waitcnt vmcnt(13)\n\t"
      "v_exp_f32 v76, v76\n\t"
      "v_mfma_f32_16x16x32_bf16 v[132:135], v[108:111], v[248:251], v[132:135]\n\t"
      "s_barrier\n\t"
      "v_cvt_pk_bf16_f32 v101, v74, v75\n\t"
      "v_mfma_f32_16x16x32_bf16 v[80:83], a[192:195], a[176:179], v[124:127]\n\t"
      "v_exp_f32 v77, v77\n\t"
      "v_mfma_f32_16x16x32_bf16 v[80:83], a[196:199], a[180:183], v[80:83]\n\t"
      "ds_read_b128 a[192:195], v136 offset:32768\n\t"
      "v_cvt_pk_bf16_f32 v102, v76, v77\n\t"
      "v_mfma_f32_16x16x32_bf16 v[80:83], a[200:203], a[184:187], v[80:83]\n\t"
      "v_exp_f32 v78, v78\n\t"
      "v_mfma_f32_16x16x32_bf16 v[80:83], a[204:207], a[188:191], v[80:83]\n\t"
      "ds_read_b128 a[196:199], v137 offset:32768\n\t"
      "v_mfma_f32_16x16x32_bf16 v[84:87], a[220:223], a[188:191], v[124:127]\n\t"
      "v_exp_f32 v79, v79\n\t"
      "v_mfma_f32_16x16x32_bf16 v[84:87], a[216:219], a[184:187], v[84:87]\n\t"
      "ds_read_b128 a[200:203], v138 offset:32768\n\t"
      "v_cvt_pk_bf16_f32 v103, v78, v79\n\t"
      "v_mfma_f32_16x16x32_bf16 v[84:87], a[212:215], a[180:183], v[84:87]\n\t"
      "buffer_load_dwordx4 v152, s[36:39], s26 offen offset:2048 sc1 lds\n\t"
      "v_mfma_f32_16x16x32_bf16 v[84:87], a[208:211], a[176:179], v[84:87]\n\t"
      "ds_read_b128 a[204:207], v139 offset:32768\n\t"
      "v_mfma_f32_16x16x32_bf16 v[88:91], a[224:227], a[176:179], v[124:127]\n\t"
      "v_exp_f32 v80, v80\n\t"
      "v_mfma_f32_16x16x32_bf16 v[88:91], a[228:231], a[180:183], v[88:91]\n\t"
      "ds_read_b128 a[208:211], v136 offset:36864\n\t"
      "v_mfma_f32_16x16x32_bf16 v[88:91], a[232:235], a[184:187], v[88:91]\n\t"
      "v_exp_f32 v81, v81\n\t"
      "v_mfma_f32_16x16x32_bf16 v[88:91], a[236:239], a[188:191], v[88:91]\n\t"
      "ds_read_b128 a[212:215], v137 offset:36864\n\t"
      "v_cvt_pk_bf16_f32 v104, v80, v81\n\t"
      "v_mfma_f32_16x16x32_bf16 v[92:95], a[252:255], a[188:191], v[124:127]\n\t"
      "v_exp_f32 v82, v82\n\t"
      "v_mfma_f32_16x16x32_bf16 v[92:95], a[248:251], a[184:187], v[92:95]\n\t"
      "ds_read_b128 a[216:219], v138 offset:36864\n\t"
      "v_mfma_f32_16x16x32_bf16 v[92:95], a[244:247], a[180:183], v[92:95]\n\t"
      "v_exp_f32 v83, v83\n\t"
      "v_mfma_f32_16x16x32_bf16 v[92:95], a[240:243], a[176:179], v[92:95]\n\t"
      "ds_read_b128 a[220:223], v139 offset:36864\n\t"
      "v_cvt_pk_bf16_f32 v105, v82, v83\n\t"
      "v_mfma_f32_16x16x32_bf16 a[8:11], v[96:99], v[0:3], a[8:11]\n\t"
      "v_exp_f32 v84, v84\n\t"
      "v_mfma_f32_16x16x32_bf16 a[8:11], v[100:103], v[4:7], a[8:11]\n\t"
      "ds_read_b128 a[224:227], v136 offset:40960\n\t"
      "v_mfma_f32_16x16x32_bf16 a[24:27], v[100:103], v[12:15], a[24:27]\n\t"
      "v_exp_f32 v85, v85\n\t"
      "v_mfma_f32_16x16x32_bf16 a[24:27], v[96:99], v[8:11], a[24:27]\n\t"
      "ds_read_b128 a[228:231], v137 offset:40960\n\t"
      "v_cvt_pk_bf16_f32 v106, v84, v85\n\t"
      "v_mfma_f32_16x16x32_bf16 a[40:43], v[96:99], v[16:19], a[40:43]\n\t"
      "v_exp_f32 v86, v86\n\t"
      "v_mfma_f32_16x16x32_bf16 a[40:43], v[100:103], v[20:23], a[40:43]\n\t"
      "ds_read_b128 a[232:235], v138 offset:40960\n\t"
      "v_mfma_f32_16x16x32_bf16 a[56:59], v[100:103], v[28:31], a[56:59]\n\t"
      "v_exp_f32 v87, v87\n\t"
      "v_mfma_f32_16x16x32_bf16 a[56:59], v[96:99], v[24:27], a[56:59]\n\t"
      "ds_read_b128 a[236:239], v139 offset:40960\n\t"
      "v_cvt_pk_bf16_f32 v107, v86, v87\n\t"
      "v_mfma_f32_16x16x32_bf16 a[72:75], v[96:99], v[32:35], a[72:75]\n\t"
      "v_exp_f32 v88, v88\n\t"
      "v_mfma_f32_16x16x32_bf16 a[72:75], v[100:103], v[36:39], a[72:75]\n\t"
      "ds_read_b128 a[240:243], v136 offset:45056\n\t"
      "v_mfma_f32_16x16x32_bf16 a[88:91], v[100:103], v[44:47], a[88:91]\n\t"
      "v_exp_f32 v89, v89\n\t"
      "v_mfma_f32_16x16x32_bf16 a[88:91], v[96:99], v[40:43], a[88:91]\n\t"
      "ds_read_b128 a[244:247], v137 offset:45056\n\t"
      "v_cvt_pk_bf16_f32 v108, v88, v89\n\t"
      "v_mfma_f32_16x16x32_bf16 a[104:107], v[96:99], v[48:51], a[104:107]\n\t"
      "v_exp_f32 v90, v90\n\t"
      "v_mfma_f32_16x16x32_bf16 a[104:107], v[100:103], v[52:55], a[104:107]\n\t"
      "ds_read_b128 a[248:251], v138 offset:45056\n\t"
      "v_mfma_f32_16x16x32_bf16 a[120:123], v[100:103], v[60:63], a[120:123]\n\t"
      "v_exp_f32 v91, v91\n\t"
      "v_mfma_f32_16x16x32_bf16 a[120:123], v[96:99], v[56:59], a[120:123]\n\t"
      "ds_read_b128 a[252:255], v139 offset:45056\n\t"
      "v_cvt_pk_bf16_f32 v109, v90, v91\n\t"
      "v_mfma_f32_16x16x32_bf16 v[140:143], v[96:99], v[248:251], v[140:143]\n\t"
      "s_waitcnt lgkmcnt(12)\n\t"
      "v_exp_f32 v92, v92\n\t"
      "v_mfma_f32_16x16x32_bf16 v[140:143], v[100:103], v[248:251], v[140:143]\n\t"
      "buffer_load_dwordx4 v152, s[36:39], s26 offen offset:3072 sc1 lds\n\t"
      ".Lattn_u1:\n\t"
      "v_mfma_f32_16x16x32_bf16 v[64:67], a[192:195], a[128:131], v[112:115]\n\t"
      "s_add_u32 s26, s26, 126976\n\t"
      "v_exp_f32 v93, v93\n\t"
      "v_mfma_f32_16x16x32_bf16 v[64:67], a[196:199], a[132:135], v[64:67]\n\t"
      "v_cvt_pk_bf16_f32 v110, v92, v93\n\t"
      "v_mfma_f32_16x16x32_bf16 v[64:67], a[200:203], a[136:139], v[64:67]\n\t"
      "s_waitcnt lgkmcnt(8)\n\t"
      "v_exp_f32 v94, v94\n\t"
      "v_mfma_f32_16x16x32_bf16 v[64:67], a[204:207], a[140:143], v[64:67]\n\t"
      "buffer_load_dwordx2 v[206:207], v159, s[60:63], s90 offen offset:0 nt\n\t"
      "v_mfma_f32_16x16x32_bf16 v[68:71], a[220:223], a[140:143], v[112:115]\n\t"
      "s_waitcnt vmcnt(10)\n\t"
      "v_exp_f32 v95, v95\n\t"
      "v_mfma_f32_16x16x32_bf16 v[68:71], a[216:219], a[136:139], v[68:71]\n\t"
      "s_waitcnt lgkmcnt(4)\n\t"
      "v_cvt_pk_bf16_f32 v111, v94, v95\n\t"
      "v_perm_b32 v224, v202, v202, v156\n\t"
      "v_mfma_f32_16x16x32_bf16 v[68:71], a[212:215], a[132:135], v[68:71]\n\t"
      "v_perm_b32 v225, v203, v203, v156\n\t"
      "v_perm_b32 v226, v204, v204, v156\n\t"
      "v_mfma_f32_16x16x32_bf16 v[68:71], a[208:211], a[128:131], v[68:71]\n\t"
      "v_perm_b32 v227, v205, v205, v156\n\t"
      "v_mul_f32 v224, s95, v224\n\t"
      "v_mfma_f32_16x16x32_bf16 v[72:75], a[224:227], a[128:131], v[112:115]\n\t"
      "v_exp_f32 v64, v64\n\t"
      "v_mfma_f32_16x16x32_bf16 v[72:75], a[228:231], a[132:135], v[72:75]\n\t"
      "v_mul_f32 v225, s95, v225\n\t"
      "v_mul_f32_e64 v226, -v226, v224\n\t"
      "v_mfma_f32_16x16x32_bf16 v[72:75], a[232:235], a[136:139], v[72:75]\n\t"
      "s_waitcnt lgkmcnt(0)\n\t"
      "v_exp_f32 v65, v65\n\t"
      "v_mfma_f32_16x16x32_bf16 v[72:75], a[236:239], a[140:143], v[72:75]\n\t"
      "v_cvt_pk_bf16_f32 v96, v64, v65\n\t"
      "v_add_u32 v136, 0x10000, v136\n\t"
      "v_mfma_f32_16x16x32_bf16 v[76:79], a[252:255], a[140:143], v[112:115]\n\t"
      "v_exp_f32 v66, v66\n\t"
      "v_mfma_f32_16x16x32_bf16 v[76:79], a[248:251], a[136:139], v[76:79]\n\t"
      "buffer_load_dwordx2 v[208:209], v159, s[60:63], s90 offen offset:512 nt\n\t"
      "v_mfma_f32_16x16x32_bf16 v[76:79], a[244:247], a[132:135], v[76:79]\n\t"
      "v_exp_f32 v67, v67\n\t"
      "v_mfma_f32_16x16x32_bf16 v[76:79], a[240:243], a[128:131], v[76:79]\n\t"
      "v_cvt_pk_bf16_f32 v97, v66, v67\n\t"
      "v_add_u32 v137, 0x10000, v137\n\t"
      "v_mfma_f32_16x16x32_bf16 a[12:15], v[104:107], v[0:3], a[12:15]\n\t"
      "v_exp_f32 v68, v68\n\t"
      "v_mfma_f32_16x16x32_bf16 a[12:15], v[108:111], v[4:7], a[12:15]\n\t"
      "ds_read_b128 v[0:3], v144 offset:49152\n\t"
      "v_add_u32 v138, 0x10000, v138\n\t"
      "v_mfma_f32_16x16x32_bf16 a[28:31], v[108:111], v[12:15], a[28:31]\n\t"
      "v_exp_f32 v69, v69\n\t"
      "v_mfma_f32_16x16x32_bf16 a[28:31], v[104:107], v[8:11], a[28:31]\n\t"
      "ds_read_b128 v[4:7], v144 offset:57344\n\t"
      "v_cvt_pk_bf16_f32 v98, v68, v69\n\t"
      "v_mfma_f32_16x16x32_bf16 a[44:47], v[104:107], v[16:19], a[44:47]\n\t"
      "v_exp_f32 v70, v70\n\t"
      "v_mfma_f32_16x16x32_bf16 a[44:47], v[108:111], v[20:23], a[44:47]\n\t"
      "ds_read_b128 v[8:11], v144 offset:50176\n\t"
      "v_add_u32 v139, 0x10000, v139\n\t"
      "v_mul_f32_e64 v227, -v227, v225\n\t"
      "v_mfma_f32_16x16x32_bf16 a[60:63], v[108:111], v[28:31], a[60:63]\n\t"
      "v_exp_f32 v71, v71\n\t"
      "v_mfma_f32_16x16x32_bf16 a[60:63], v[104:107], v[24:27], a[60:63]\n\t"
      "ds_read_b128 v[12:15], v144 offset:58368\n\t"
      "v_cvt_pk_bf16_f32 v99, v70, v71\n\t"
      "v_mfma_f32_16x16x32_bf16 a[76:79], v[104:107], v[32:35], a[76:79]\n\t"
      "buffer_load_dwordx2 v[210:211], v159, s[60:63], s90 offen offset:1024 nt\n\t"
      "v_exp_f32 v72, v72\n\t"
      "v_mfma_f32_16x16x32_bf16 a[76:79], v[108:111], v[36:39], a[76:79]\n\t"
      "ds_read_b128 v[16:19], v145 offset:49152\n\t"
      "v_cvt_f32_i32 v170, v170\n\t"
      "v_mfma_f32_16x16x32_bf16 a[92:95], v[108:111], v[44:47], a[92:95]\n\t"
      "v_exp_f32 v73, v73\n\t"
      "v_mfma_f32_16x16x32_bf16 a[92:95], v[104:107], v[40:43], a[92:95]\n\t"
      "ds_read_b128 v[20:23], v145 offset:57344\n\t"
      "v_cvt_pk_bf16_f32 v100, v72, v73\n\t"
      "v_mfma_f32_16x16x32_bf16 a[108:111], v[104:107], v[48:51], a[108:111]\n\t"
      "v_exp_f32 v74, v74\n\t"
      "v_mfma_f32_16x16x32_bf16 a[108:111], v[108:111], v[52:55], a[108:111]\n\t"
      "ds_read_b128 v[24:27], v145 offset:50176\n\t"
      "v_cvt_f32_i32 v178, v178\n\t"
      "v_mfma_f32_16x16x32_bf16 a[124:127], v[108:111], v[60:63], a[124:127]\n\t"
      "v_exp_f32 v75, v75\n\t"
      "v_mfma_f32_16x16x32_bf16 a[124:127], v[104:107], v[56:59], a[124:127]\n\t"
      "ds_read_b128 v[28:31], v145 offset:58368\n\t"
      "v_cvt_pk_bf16_f32 v101, v74, v75\n\t"
      "v_mfma_f32_16x16x32_bf16 v[148:151], v[104:107], v[248:251], v[148:151]\n\t"
      "v_exp_f32 v76, v76\n\t"
      "v_mfma_f32_16x16x32_bf16 v[148:151], v[108:111], v[248:251], v[148:151]\n\t"
      "ds_read_b128 v[32:35], v146 offset:49152\n\t"
      "v_fma_f32 v170, v170, v224, v226\n\t"
      "v_mfma_f32_16x16x32_bf16 v[80:83], a[192:195], a[144:147], v[116:119]\n\t"
      "v_exp_f32 v77, v77\n\t"
      "v_mfma_f32_16x16x32_bf16 v[80:83], a[196:199], a[148:151], v[80:83]\n\t"
      "ds_read_b128 v[36:39], v146 offset:57344\n\t"
      "v_cvt_pk_bf16_f32 v102, v76, v77\n\t"
      "v_mfma_f32_16x16x32_bf16 v[80:83], a[200:203], a[152:155], v[80:83]\n\t"
      "v_exp_f32 v78, v78\n\t"
      "v_mfma_f32_16x16x32_bf16 v[80:83], a[204:207], a[156:159], v[80:83]\n\t"
      "ds_read_b128 v[40:43], v146 offset:50176\n\t"
      "v_fma_f32 v178, v178, v225, v227\n\t"
      "v_mfma_f32_16x16x32_bf16 v[84:87], a[220:223], a[156:159], v[116:119]\n\t"
      "v_exp_f32 v79, v79\n\t"
      "v_mfma_f32_16x16x32_bf16 v[84:87], a[216:219], a[152:155], v[84:87]\n\t"
      "ds_read_b128 v[44:47], v146 offset:58368\n\t"
      "v_cvt_pk_bf16_f32 v103, v78, v79\n\t"
      "v_mfma_f32_16x16x32_bf16 v[84:87], a[212:215], a[148:151], v[84:87]\n\t"
      "buffer_load_dwordx2 v[212:213], v159, s[60:63], s90 offen offset:1536 nt\n\t"
      "v_mfma_f32_16x16x32_bf16 v[84:87], a[208:211], a[144:147], v[84:87]\n\t"
      "ds_read_b128 v[48:51], v147 offset:49152\n\t"
      "v_lshlrev_b32 v228, 16, v186\n\t"
      "v_mfma_f32_16x16x32_bf16 v[88:91], a[224:227], a[144:147], v[116:119]\n\t"
      "v_exp_f32 v80, v80\n\t"
      "v_mfma_f32_16x16x32_bf16 v[88:91], a[228:231], a[148:151], v[88:91]\n\t"
      "ds_read_b128 v[52:55], v147 offset:57344\n\t"
      "v_lshlrev_b32 v229, 16, v190\n\t"
      "v_mfma_f32_16x16x32_bf16 v[88:91], a[232:235], a[152:155], v[88:91]\n\t"
      "v_exp_f32 v81, v81\n\t"
      "v_mfma_f32_16x16x32_bf16 v[88:91], a[236:239], a[156:159], v[88:91]\n\t"
      "ds_read_b128 v[56:59], v147 offset:50176\n\t"
      "v_cvt_pk_bf16_f32 v104, v80, v81\n\t"
      "v_mfma_f32_16x16x32_bf16 v[92:95], a[252:255], a[156:159], v[116:119]\n\t"
      "v_exp_f32 v82, v82\n\t"
      "v_mfma_f32_16x16x32_bf16 v[92:95], a[248:251], a[152:155], v[92:95]\n\t"
      "ds_read_b128 v[60:63], v147 offset:58368\n\t"
      "v_lshlrev_b32 v230, 16, v194\n\t"
      "v_mfma_f32_16x16x32_bf16 v[92:95], a[244:247], a[148:151], v[92:95]\n\t"
      "v_exp_f32 v83, v83\n\t"
      "v_mfma_f32_16x16x32_bf16 v[92:95], a[240:243], a[144:147], v[92:95]\n\t"
      "s_waitcnt lgkmcnt(8)\n\t"
      "v_cvt_pk_bf16_f32 v105, v82, v83\n\t"
      "v_lshlrev_b32 v231, 16, v198\n\t"
      "v_mfma_f32_16x16x32_bf16 a[0:3], v[96:99], v[0:3], a[0:3]\n\t"
      "v_exp_f32 v84, v84\n\t"
      "v_mfma_f32_16x16x32_bf16 a[0:3], v[100:103], v[4:7], a[0:3]\n\t"
      "v_mul_f32 v228, v170, v228\n\t"
      "v_mul_f32 v229, v178, v229\n\t"
      "v_mfma_f32_16x16x32_bf16 a[16:19], v[100:103], v[12:15], a[16:19]\n\t"
      "v_exp_f32 v85, v85\n\t"
      "v_mfma_f32_16x16x32_bf16 a[16:19], v[96:99], v[8:11], a[16:19]\n\t"
      "v_cvt_pk_bf16_f32 v106, v84, v85\n\t"
      "v_fma_f32 v228, -v178, v230, v228\n\t"
      "v_mfma_f32_16x16x32_bf16 a[32:35], v[96:99], v[16:19], a[32:35]\n\t"
      "v_exp_f32 v86, v86\n\t"
      "v_mfma_f32_16x16x32_bf16 a[32:35], v[100:103], v[20:23], a[32:35]\n\t"
      "v_fmac_f32 v229, v170, v231\n\t"
      "v_cvt_f32_i32 v171, v171\n\t"
      "v_mfma_f32_16x16x32_bf16 a[48:51], v[100:103], v[28:31], a[48:51]\n\t"
      "s_waitcnt lgkmcnt(0)\n\t"
      "v_exp_f32 v87, v87\n\t"
      "v_mfma_f32_16x16x32_bf16 a[48:51], v[96:99], v[24:27], a[48:51]\n\t"
      "v_cvt_pk_bf16_f32 v107, v86, v87\n\t"
      "v_cvt_f32_i32 v179, v179\n\t"
      "v_mfma_f32_16x16x32_bf16 a[64:67], v[96:99], v[32:35], a[64:67]\n\t"
      "s_add_u32 m0, s85, 98304\n\t"
      "v_exp_f32 v88, v88\n\t"
      "v_mfma_f32_16x16x32_bf16 a[64:67], v[100:103], v[36:39], a[64:67]\n\t"
      "buffer_load_dwordx4 v152, s[36:39], s27 offen offset:0 sc1 lds\n\t"
      "v_mfma_f32_16x16x32_bf16 a[80:83], v[100:103], v[44:47], a[80:83]\n\t"
      "v_exp_f32 v89, v89\n\t"
      "v_mfma_f32_16x16x32_bf16 a[80:83], v[96:99], v[40:43], a[80:83]\n\t"
      "v_cvt_pk_bf16_f32 v108, v88, v89\n\t"
      "v_add_u32 v144, 0x10000, v144\n\t"
      "v_fma_f32 v171, v171, v224, v226\n\t"
      "v_mfma_f32_16x16x32_bf16 a[96:99], v[96:99], v[48:51], a[96:99]\n\t"
      "v_exp_f32 v90, v90\n\t"
      "v_mfma_f32_16x16x32_bf16 a[96:99], v[100:103], v[52:55], a[96:99]\n\t"
      "v_add_u32 v145, 0x10000, v145\n\t"
      "v_add_u32 v146, 0x10000, v146\n\t"
      "v_mfma_f32_16x16x32_bf16 a[112:115], v[100:103], v[60:63], a[112:115]\n\t"
      "v_exp_f32 v91, v91\n\t"
      "v_mfma_f32_16x16x32_bf16 a[112:115], v[96:99], v[56:59], a[112:115]\n\t"
      "v_cvt_pk_bf16_f32 v109, v90, v91\n\t"
      "v_add_u32 v147, 0x10000, v147\n\t"
      "v_mfma_f32_16x16x32_bf16 v[128:131], v[96:99], v[248:251], v[128:131]\n\t"
      "v_exp_f32 v92, v92\n\t"
      "v_mfma_f32_16x16x32_bf16 v[128:131], v[100:103], v[248:251], v[128:131]\n\t"
      "buffer_load_dwordx4 v152, s[36:39], s27 offen offset:1024 sc1 lds\n\t"
      "v_mfma_f32_16x16x32_bf16 v[64:67], a[192:195], a[160:163], v[120:123]\n\t"
      "v_exp_f32 v93, v93\n\t"
      "v_mfma_f32_16x16x32_bf16 v[64:67], a[196:199], a[164:167], v[64:67]\n\t"
      "v_cvt_pk_bf16_f32 v110, v92, v93\n\t"
      "v_fma_f32 v179, v179, v225, v227\n\t"
      "v_mfma_f32_16x16x32_bf16 v[64:67], a[200:203], a[168:171], v[64:67]\n\t"
      "v_exp_f32 v94, v94\n\t"
      "v_mfma_f32_16x16x32_bf16 v[64:67], a[204:207], a[172:175], v[64:67]\n\t"
      "v_and_b32 v232, s89, v186\n\t"
      "v_and_b32 v233, s89, v190\n\t"
      "v_mfma_f32_16x16x32_bf16 v[68:71], a[220:223], a[172:175], v[120:123]\n\t"
      "v_exp_f32 v95, v95\n\t"
      "v_mfma_f32_16x16x32_bf16 v[68:71], a[216:219], a[168:171], v[68:71]\n\t"
      "v_cvt_pk_bf16_f32 v111, v94, v95\n\t"
      "v_and_b32 v234, s89, v194\n\t"
      "v_mfma_f32_16x16x32_bf16 v[68:71], a[212:215], a[164:167], v[68:71]\n\t"
      "buffer_load_dwordx4 v152, s[36:39], s27 offen offset:2048 sc1 lds\n\t"
      "v_mfma_f32_16x16x32_bf16 v[68:71], a[208:211], a[160:163], v[68:71]\n\t"
      "v_and_b32 v235, s89, v198\n\t"
      "v_mul_f32 v232, v171, v232\n\t"
      "v_mfma_f32_16x16x32_bf16 v[72:75], a[224:227], a[160:163], v[120:123]\n\t"
      "v_exp_f32 v64, v64\n\t"
      "v_mfma_f32_16x16x32_bf16 v[72:75], a[228:231], a[164:167], v[72:75]\n\t"
      "v_mul_f32 v233, v179, v233\n\t"
      "v_fma_f32 v232, -v179, v234, v232\n\t"
      "v_mfma_f32_16x16x32_bf16 v[72:75], a[232:235], a[168:171], v[72:75]\n\t"
      "v_exp_f32 v65, v65\n\t"
      "v_mfma_f32_16x16x32_bf16 v[72:75], a[236:239], a[172:175], v[72:75]\n\t"
      "v_cvt_pk_bf16_f32 v96, v64, v65\n\t"
      "v_fmac_f32 v233, v171, v235\n\t"
      "v_mfma_f32_16x16x32_bf16 v[76:79], a[252:255], a[172:175], v[120:123]\n\t"
      "v_exp_f32 v66, v66\n\t"
      "v_mfma_f32_16x16x32_bf16 v[76:79], a[248:251], a[168:171], v[76:79]\n\t"
      "v_cvt_pk_bf16_f32 v236, v228, v232\n\t"
      "v_cvt_pk_bf16_f32 v240, v229, v233\n\t"
      "v_mfma_f32_16x16x32_bf16 v[76:79], a[244:247], a[164:167], v[76:79]\n\t"
      "v_exp_f32 v67, v67\n\t"
      "v_mfma_f32_16x16x32_bf16 v[76:79], a[240:243], a[160:163], v[76:79]\n\t"
      "buffer_load_dwordx4 v152, s[36:39], s27 offen offset:3072 sc1 lds\n\t"
      "v_mfma_f32_16x16x32_bf16 a[4:7], v[104:107], v[0:3], a[4:7]\n\t"
      "s_add_u32 s27, s27, 4096\n\t"
      "v_exp_f32 v68, v68\n\t"
      "v_mfma_f32_16x16x32_bf16 a[4:7], v[108:111], v[4:7], a[4:7]\n\t"
      "v_cvt_pk_bf16_f32 v97, v66, v67\n\t"
      "v_cvt_f32_i32 v172, v172\n\t"
      "v_mfma_f32_16x16x32_bf16 a[20:23], v[108:111], v[12:15], a[20:23]\n\t"
      "s_add_u32 m0, s85, 102400\n\t"
      "v_exp_f32 v69, v69\n\t"
      "v_mfma_f32_16x16x32_bf16 a[20:23], v[104:107], v[8:11], a[20:23]\n\t"
      "v_cvt_pk_bf16_f32 v98, v68, v69\n\t"
      "v_cvt_f32_i32 v180, v180\n\t"
      "v_mfma_f32_16x16x32_bf16 a[36:39], v[104:107], v[16:19], a[36:39]\n\t"
      "v_exp_f32 v70, v70\n\t"
      "v_mfma_f32_16x16x32_bf16 a[36:39], v[108:111], v[20:23], a[36:39]\n\t"
      "v_fma_f32 v172, v172, v224, v226\n\t"
      "v_fma_f32 v180, v180, v225, v227\n\t"
      "v_mfma_f32_16x16x32_bf16 a[52:55], v[108:111], v[28:31], a[52:55]\n\t"
      "v_exp_f32 v71, v71\n\t"
      "v_mfma_f32_16x16x32_bf16 a[52:55], v[104:107], v[24:27], a[52:55]\n\t"
      "buffer_load_dwordx4 v152, s[36:39], s27 offen offset:0 sc1 lds\n\t"
      "v_mfma_f32_16x16x32_bf16 a[68:71], v[104:107], v[32:35], a[68:71]\n\t"
      "v_exp_f32 v72, v72\n\t"
      "v_mfma_f32_16x16x32_bf16 a[68:71], v[108:111], v[36:39], a[68:71]\n\t"
      "v_cvt_pk_bf16_f32 v99, v70, v71\n\t"
      "v_lshlrev_b32 v228, 16, v187\n\t"
      "v_mfma_f32_16x16x32_bf16 a[84:87], v[108:111], v[44:47], a[84:87]\n\t"
      "v_exp_f32 v73, v73\n\t"
      "v_mfma_f32_16x16x32_bf16 a[84:87], v[104:107], v[40:43], a[84:87]\n\t"
      "v_cvt_pk_bf16_f32 v100, v72, v73\n\t"
      "v_lshlrev_b32 v229, 16, v191\n\t"
      "v_mfma_f32_16x16x32_bf16 a[100:103], v[104:107], v[48:51], a[100:103]\n\t"
      "v_exp_f32 v74, v74\n\t"
      "v_mfma_f32_16x16x32_bf16 a[100:103], v[108:111], v[52:55], a[100:103]\n\t"
      "v_lshlrev_b32 v230, 16, v195\n\t"
      "v_lshlrev_b32 v231, 16, v199\n\t"
      "v_mfma_f32_16x16x32_bf16 a[116:119], v[108:111], v[60:63], a[116:119]\n\t"
      "v_exp_f32 v75, v75\n\t"
      "v_mfma_f32_16x16x32_bf16 a[116:119], v[104:107], v[56:59], a[116:119]\n\t"
      "buffer_load_dwordx4 v152, s[36:39], s27 offen offset:1024 sc1 lds\n\t"
      "v_mfma_f32_16x16x32_bf16 v[132:135], v[104:107], v[248:251], v[132:135]\n\t"
      "s_waitcnt vmcnt(10)\n\t"
      "v_exp_f32 v76, v76\n\t"
      "v_mfma_f32_16x16x32_bf16 v[132:135], v[108:111], v[248:251], v[132:135]\n\t"
      "s_barrier\n\t"
      "v_cvt_pk_bf16_f32 v101, v74, v75\n\t"
      "v_mul_f32 v228, v172, v228\n\t"
      "v_mul_f32 v229, v180, v229\n\t"
      "v_mfma_f32_16x16x32_bf16 v[80:83], a[192:195], a[176:179], v[124:127]\n\t"
      "v_exp_f32 v77, v77\n\t"
      "v_mfma_f32_16x16x32_bf16 v[80:83], a[196:199], a[180:183], v[80:83]\n\t"
      "ds_read_b128 a[192:195], v136 offset:0\n\t"
      "v_cvt_pk_bf16_f32 v102, v76, v77\n\t"
      "v_mfma_f32_16x16x32_bf16 v[80:83], a[200:203], a[184:187], v[80:83]\n\t"
      "v_exp_f32 v78, v78\n\t"
      "v_mfma_f32_16x16x32_bf16 v[80:83], a[204:207], a[188:191], v[80:83]\n\t"
      "ds_read_b128 a[196:199], v137 offset:0\n\t"
      "v_fma_f32 v228, -v180, v230, v228\n\t"
      "v_mfma_f32_16x16x32_bf16 v[84:87], a[220:223], a[188:191], v[124:127]\n\t"
      "v_exp_f32 v79, v79\n\t"
      "v_mfma_f32_16x16x32_bf16 v[84:87], a[216:219], a[184:187], v[84:87]\n\t"
      "ds_read_b128 a[200:203], v138 offset:0\n\t"
      "v_cvt_pk_bf16_f32 v103, v78, v79\n\t"
      "v_mfma_f32_16x16x32_bf16 v[84:87], a[212:215], a[180:183], v[84:87]\n\t"
      "buffer_load_dwordx4 v152, s[36:39], s27 offen offset:2048 sc1 lds\n\t"
      "v_mfma_f32_16x16x32_bf16 v[84:87], a[208:211], a[176:179], v[84:87]\n\t"
      "ds_read_b128 a[204:207], v139 offset:0\n\t"
      "v_fmac_f32 v229, v172, v231\n\t"
      "v_mfma_f32_16x16x32_bf16 v[88:91], a[224:227], a[176:179], v[124:127]\n\t"
      "v_exp_f32 v80, v80\n\t"
      "v_mfma_f32_16x16x32_bf16 v[88:91], a[228:231], a[180:183], v[88:91]\n\t"
      "ds_read_b128 a[208:211], v136 offset:4096\n\t"
      "v_cvt_f32_i32 v173, v173\n\t"
      "v_mfma_f32_16x16x32_bf16 v[88:91], a[232:235], a[184:187], v[88:91]\n\t"
      "buffer_load_dwordx2 v[166:167], v162, s[36:39], s94 offen sc1\n\t"
      "v_exp_f32 v81, v81\n\t"
      "v_mfma_f32_16x16x32_bf16 v[88:91], a[236:239], a[188:191], v[88:91]\n\t"
      "ds_read_b128 a[212:215], v137 offset:4096\n\t"
      "v_cvt_pk_bf16_f32 v104, v80, v81\n\t"
      "v_mfma_f32_16x16x32_bf16 v[92:95], a[252:255], a[188:191], v[124:127]\n\t"
      "v_exp_f32 v82, v82\n\t"
      "v_mfma_f32_16x16x32_bf16 v[92:95], a[248:251], a[184:187], v[92:95]\n\t"
      "ds_read_b128 a[216:219], v138 offset:4096\n\t"
      "v_cvt_f32_i32 v181, v181\n\t"
      "v_mfma_f32_16x16x32_bf16 v[92:95], a[244:247], a[180:183], v[92:95]\n\t"
      "v_exp_f32 v83, v83\n\t"
      "v_mfma_f32_16x16x32_bf16 v[92:95], a[240:243], a[176:179], v[92:95]\n\t"
      "ds_read_b128 a[220:223], v139 offset:4096\n\t"
      "v_cvt_pk_bf16_f32 v105, v82, v83\n\t"
      "v_mfma_f32_16x16x32_bf16 a[8:11], v[96:99], v[0:3], a[8:11]\n\t"
      "buffer_load_dwordx2 v[214:215], v159, s[60:63], s90 offen offset:2048 nt\n\t"
      "v_exp_f32 v84, v84\n\t"
      "v_mfma_f32_16x16x32_bf16 a[8:11], v[100:103], v[4:7], a[8:11]\n\t"
      "ds_read_b128 a[224:227], v136 offset:8192\n\t"
      "v_fma_f32 v173, v173, v224, v226\n\t"
      "v_mfma_f32_16x16x32_bf16 a[24:27], v[100:103], v[12:15], a[24:27]\n\t"
      "v_exp_f32 v85, v85\n\t"
      "v_mfma_f32_16x16x32_bf16 a[24:27], v[96:99], v[8:11], a[24:27]\n\t"
      "ds_read_b128 a[228:231], v137 offset:8192\n\t"
      "v_cvt_pk_bf16_f32 v106, v84, v85\n\t"
      "v_mfma_f32_16x16x32_bf16 a[40:43], v[96:99], v[16:19], a[40:43]\n\t"
      "v_exp_f32 v86, v86\n\t"
      "v_mfma_f32_16x16x32_bf16 a[40:43], v[100:103], v[20:23], a[40:43]\n\t"
      "ds_read_b128 a[232:235], v138 offset:8192\n\t"
      "v_fma_f32 v181, v181, v225, v227\n\t"
      "v_mfma_f32_16x16x32_bf16 a[56:59], v[100:103], v[28:31], a[56:59]\n\t"
      "v_exp_f32 v87, v87\n\t"
      "v_mfma_f32_16x16x32_bf16 a[56:59], v[96:99], v[24:27], a[56:59]\n\t"
      "ds_read_b128 a[236:239], v139 offset:8192\n\t"
      "v_cvt_pk_bf16_f32 v107, v86, v87\n\t"
      "v_mfma_f32_16x16x32_bf16 a[72:75], v[96:99], v[32:35], a[72:75]\n\t"
      "buffer_load_dwordx2 v[216:217], v159, s[60:63], s90 offen offset:2560 nt\n\t"
      "v_exp_f32 v88, v88\n\t"
      "v_mfma_f32_16x16x32_bf16 a[72:75], v[100:103], v[36:39], a[72:75]\n\t"
      "ds_read_b128 a[240:243], v136 offset:12288\n\t"
      "v_and_b32 v232, s89, v187\n\t"
      "v_mfma_f32_16x16x32_bf16 a[88:91], v[100:103], v[44:47], a[88:91]\n\t"
      "v_exp_f32 v89, v89\n\t"
      "v_mfma_f32_16x16x32_bf16 a[88:91], v[96:99], v[40:43], a[88:91]\n\t"
      "ds_read_b128 a[244:247], v137 offset:12288\n\t"
      "v_cvt_pk_bf16_f32 v108, v88, v89\n\t"
      "v_mfma_f32_16x16x32_bf16 a[104:107], v[96:99], v[48:51], a[104:107]\n\t"
      "v_exp_f32 v90, v90\n\t"
      "v_mfma_f32_16x16x32_bf16 a[104:107], v[100:103], v[52:55], a[104:107]\n\t"
      "ds_read_b128 a[248:251], v138 offset:12288\n\t"
      "v_and_b32 v233, s89, v191\n\t"
      "v_mfma_f32_16x16x32_bf16 a[120:123], v[100:103], v[60:63], a[120:123]\n\t"
      "v_exp_f32 v91, v91\n\t"
      "v_mfma_f32_16x16x32_bf16 a[120:123], v[96:99], v[56:59], a[120:123]\n\t"
      "ds_read_b128 a[252:255], v139 offset:12288\n\t"
      "v_cvt_pk_bf16_f32 v109, v90, v91\n\t"
      "v_mfma_f32_16x16x32_bf16 v[140:143], v[96:99], v[248:251], v[140:143]\n\t"
      "s_waitcnt lgkmcnt(12)\n\t"
      "v_exp_f32 v92, v92\n\t"
      "v_mfma_f32_16x16x32_bf16 v[140:143], v[100:103], v[248:251], v[140:143]\n\t"
      "buffer_load_dwordx4 v152, s[36:39], s27 offen offset:3072 sc1 lds\n\t"
      "v_mfma_f32_16x16x32_bf16 v[64:67], a[192:195], a[128:131], v[112:115]\n\t"
      "s_add_u32 s27, s27, 126976\n\t"
      "v_exp_f32 v93, v93\n\t"
      "v_mfma_f32_16x16x32_bf16 v[64:67], a[196:199], a[132:135], v[64:67]\n\t"
      "v_cvt_pk_bf16_f32 v110, v92, v93\n\t"
      "v_and_b32 v234, s89, v195\n\t"
      "v_and_b32 v235, s89, v199\n\t"
      "v_mfma_f32_16x16x32_bf16 v[64:67], a[200:203], a[136:139], v[64:67]\n\t"
      "s_waitcnt lgkmcnt(8)\n\t"
      "v_exp_f32 v94, v94\n\t"
      "v_mfma_f32_16x16x32_bf16 v[64:67], a[204:207], a[140:143], v[64:67]\n\t"
      "buffer_load_dwordx2 v[218:219], v159, s[60:63], s90 offen offset:3072 nt\n\t"
      "v_mfma_f32_16x16x32_bf16 v[68:71], a[220:223], a[140:143], v[112:115]\n\t"
      "v_exp_f32 v95, v95\n\t"
      "v_mfma_f32_16x16x32_bf16 v[68:71], a[216:219], a[136:139], v[68:71]\n\t"
      "s_waitcnt lgkmcnt(4)\n\t"
      "v_cvt_pk_bf16_f32 v111, v94, v95\n\t"
      "v_mul_f32 v232, v173, v232\n\t"
      "v_mfma_f32_16x16x32_bf16 v[68:71], a[212:215], a[132:135], v[68:71]\n\t"
      "v_mul_f32 v233, v181, v233\n\t"
      "v_fma_f32 v232, -v181, v234, v232\n\t"
      "v_mfma_f32_16x16x32_bf16 v[68:71], a[208:211], a[128:131], v[68:71]\n\t"
      "v_fmac_f32 v233, v173, v235\n\t"
      "v_cvt_pk_bf16_f32 v237, v228, v232\n\t"
      "v_mfma_f32_16x16x32_bf16 v[72:75], a[224:227], a[128:131], v[112:115]\n\t"
      "v_exp_f32 v64, v64\n\t"
      "v_mfma_f32_16x16x32_bf16 v[72:75], a[228:231], a[132:135], v[72:75]\n\t"
      "v_cvt_pk_bf16_f32 v241, v229, v233\n\t"
      "v_cvt_f32_i32 v174, v174\n\t"
      "v_mfma_f32_16x16x32_bf16 v[72:75], a[232:235], a[136:139], v[72:75]\n\t"
      "s_waitcnt lgkmcnt(0)\n\t"
      "v_exp_f32 v65, v65\n\t"
      "v_mfma_f32_16x16x32_bf16 v[72:75], a[236:239], a[140:143], v[72:75]\n\t"
      "v_cvt_pk_bf16_f32 v96, v64, v65\n\t"
      "v_cvt_f32_i32 v182, v182\n\t"
      "v_mfma_f32_16x16x32_bf16 v[76:79], a[252:255], a[140:143], v[112:115]\n\t"
      "v_exp_f32 v66, v66\n\t"
      "v_mfma_f32_16x16x32_bf16 v[76:79], a[248:251], a[136:139], v[76:79]\n\t"
      "buffer_load_dwordx2 v[220:221], v159, s[60:63], s90 offen offset:3584 nt\n\t"
      "v_mfma_f32_16x16x32_bf16 v[76:79], a[244:247], a[132:135], v[76:79]\n\t"
      "v_exp_f32 v67, v67\n\t"
      "v_mfma_f32_16x16x32_bf16 v[76:79], a[240:243], a[128:131], v[76:79]\n\t"
      "v_cvt_pk_bf16_f32 v97, v66, v67\n\t"
      "v_fma_f32 v174, v174, v224, v226\n\t"
      "v_mfma_f32_16x16x32_bf16 a[12:15], v[104:107], v[0:3], a[12:15]\n\t"
      "v_exp_f32 v68, v68\n\t"
      "v_mfma_f32_16x16x32_bf16 a[12:15], v[108:111], v[4:7], a[12:15]\n\t"
      "ds_read_b128 v[0:3], v144 offset:16384\n\t"
      "v_fma_f32 v182, v182, v225, v227\n\t"
      "v_mfma_f32_16x16x32_bf16 a[28:31], v[108:111], v[12:15], a[28:31]\n\t"
      "v_exp_f32 v69, v69\n\t"
      "v_mfma_f32_16x16x32_bf16 a[28:31], v[104:107], v[8:11], a[28:31]\n\t"
      "ds_read_b128 v[4:7], v144 offset:24576\n\t"
      "v_cvt_pk_bf16_f32 v98, v68, v69\n\t"
      "v_mfma_f32_16x16x32_bf16 a[44:47], v[104:107], v[16:19], a[44:47]\n\t"
      "v_exp_f32 v70, v70\n\t"
      "v_mfma_f32_16x16x32_bf16 a[44:47], v[108:111], v[20:23], a[44:47]\n\t"
      "ds_read_b128 v[8:11], v144 offset:17408\n\t"
      "v_lshlrev_b32 v228, 16, v188\n\t"
      "v_mfma_f32_16x16x32_bf16 a[60:63], v[108:111], v[28:31], a[60:63]\n\t"
      "buffer_load_ushort v222, v160, s[64:67], s92 offen\n\t"
      "v_exp_f32 v71, v71\n\t"
      "v_mfma_f32_16x16x32_bf16 a[60:63], v[104:107], v[24:27], a[60:63]\n\t"
      "ds_read_b128 v[12:15], v144 offset:25600\n\t"
      "v_cvt_pk_bf16_f32 v99, v70, v71\n\t"
      "v_mfma_f32_16x16x32_bf16 a[76:79], v[104:107], v[32:35], a[76:79]\n\t"
      "v_exp_f32 v72, v72\n\t"
      "v_mfma_f32_16x16x32_bf16 a[76:79], v[108:111], v[36:39], a[76:79]\n\t"
      "ds_read_b128 v[16:19], v145 offset:16384\n\t"
      "v_lshlrev_b32 v229, 16, v192\n\t"
      "v_mfma_f32_16x16x32_bf16 a[92:95], v[108:111], v[44:47], a[92:95]\n\t"
      "v_exp_f32 v73, v73\n\t"
      "v_mfma_f32_16x16x32_bf16 a[92:95], v[104:107], v[40:43], a[92:95]\n\t"
      "ds_read_b128 v[20:23], v145 offset:24576\n\t"
      "v_cvt_pk_bf16_f32 v100, v72, v73\n\t"
      "v_mfma_f32_16x16x32_bf16 a[108:111], v[104:107], v[48:51], a[108:111]\n\t"
      "v_exp_f32 v74, v74\n\t"
      "v_mfma_f32_16x16x32_bf16 a[108:111], v[108:111], v[52:55], a[108:111]\n\t"
      "ds_read_b128 v[24:27], v145 offset:17408\n\t"
      "v_lshlrev_b32 v230, 16, v196\n\t"
      "v_mfma_f32_16x16x32_bf16 a[124:127], v[108:111], v[60:63], a[124:127]\n\t"
      "v_exp_f32 v75, v75\n\t"
      "v_mfma_f32_16x16x32_bf16 a[124:127], v[104:107], v[56:59], a[124:127]\n\t"
      "ds_read_b128 v[28:31], v145 offset:25600\n\t"
      "v_cvt_pk_bf16_f32 v101, v74, v75\n\t"
      "v_mfma_f32_16x16x32_bf16 v[148:151], v[104:107], v[248:251], v[148:151]\n\t"
      "buffer_load_ushort v223, v160, s[68:71], s92 offen\n\t"
      "v_exp_f32 v76, v76\n\t"
      "v_mfma_f32_16x16x32_bf16 v[148:151], v[108:111], v[248:251], v[148:151]\n\t"
      "ds_read_b128 v[32:35], v146 offset:16384\n\t"
      "v_lshlrev_b32 v231, 16, v200\n\t"
      "v_mfma_f32_16x16x32_bf16 v[80:83], a[192:195], a[144:147], v[116:119]\n\t"
      "v_exp_f32 v77, v77\n\t"
      "v_mfma_f32_16x16x32_bf16 v[80:83], a[196:199], a[148:151], v[80:83]\n\t"
      "ds_read_b128 v[36:39], v146 offset:24576\n\t"
      "v_cvt_pk_bf16_f32 v102, v76, v77\n\t"
      "v_mfma_f32_16x16x32_bf16 v[80:83], a[200:203], a[152:155], v[80:83]\n\t"
      "v_exp_f32 v78, v78\n\t"
      "v_mfma_f32_16x16x32_bf16 v[80:83], a[204:207], a[156:159], v[80:83]\n\t"
      "ds_read_b128 v[40:43], v146 offset:17408\n\t"
      "v_mul_f32 v228, v174, v228\n\t"
      "v_mfma_f32_16x16x32_bf16 v[84:87], a[220:223], a[156:159], v[116:119]\n\t"
      "v_exp_f32 v79, v79\n\t"
      "v_mfma_f32_16x16x32_bf16 v[84:87], a[216:219], a[152:155], v[84:87]\n\t"
      "ds_read_b128 v[44:47], v146 offset:25600\n\t"
      "v_cvt_pk_bf16_f32 v103, v78, v79\n\t"
      "v_mfma_f32_16x16x32_bf16 v[84:87], a[212:215], a[148:151], v[84:87]\n\t"
      "v_mul_f32 v229, v182, v229\n\t"
      "v_fma_f32 v228, -v182, v230, v228\n\t"
      "v_fmac_f32 v229, v174, v231\n\t"
      "v_mfma_f32_16x16x32_bf16 v[84:87], a[208:211], a[144:147], v[84:87]\n\t"
      "ds_read_b128 v[48:51], v147 offset:16384\n\t"
      "v_cvt_f32_i32 v175, v175\n\t"
      "v_mfma_f32_16x16x32_bf16 v[88:91], a[224:227], a[144:147], v[116:119]\n\t"
      "s_add_u32 s90, s90, 131072\n\t"
      "v_exp_f32 v80, v80\n\t"
      "v_mfma_f32_16x16x32_bf16 v[88:91], a[228:231], a[148:151], v[88:91]\n\t"
      "ds_read_b128 v[52:55], v147 offset:24576\n\t"
      "v_cvt_f32_i32 v183, v183\n\t"
      "v_mfma_f32_16x16x32_bf16 v[88:91], a[232:235], a[152:155], v[88:91]\n\t"
      "s_add_u32 s91, s91, 65536\n\t"
      "v_exp_f32 v81, v81\n\t"
      "v_mfma_f32_16x16x32_bf16 v[88:91], a[236:239], a[156:159], v[88:91]\n\t"
      "ds_read_b128 v[56:59], v147 offset:17408\n\t"
      "v_cvt_pk_bf16_f32 v104, v80, v81\n\t"
      "v_mfma_f32_16x16x32_bf16 v[92:95], a[252:255], a[156:159], v[116:119]\n\t"
      "s_add_u32 s92, s92, 2048\n\t"
      "v_exp_f32 v82, v82\n\t"
      "v_mfma_f32_16x16x32_bf16 v[92:95], a[248:251], a[152:155], v[92:95]\n\t"
      "ds_read_b128 v[60:63], v147 offset:25600\n\t"
      "v_fma_f32 v175, v175, v224, v226\n\t"
      "v_mfma_f32_16x16x32_bf16 v[92:95], a[244:247], a[148:151], v[92:95]\n\t"
      "v_exp_f32 v83, v83\n\t"
      "v_mfma_f32_16x16x32_bf16 v[92:95], a[240:243], a[144:147], v[92:95]\n\t"
      "s_waitcnt lgkmcnt(8)\n\t"
      "v_cvt_pk_bf16_f32 v105, v82, v83\n\t"
      "v_fma_f32 v183, v183, v225, v227\n\t"
      "v_mfma_f32_16x16x32_bf16 a[0:3], v[96:99], v[0:3], a[0:3]\n\t"
      "v_exp_f32 v84, v84\n\t"
      "v_mfma_f32_16x16x32_bf16 a[0:3], v[100:103], v[4:7], a[0:3]\n\t"
      "v_and_b32 v232, s89, v188\n\t"
      "v_and_b32 v233, s89, v192\n\t"
      "v_mfma_f32_16x16x32_bf16 a[16:19], v[100:103], v[12:15], a[16:19]\n\t"
      "v_exp_f32 v85, v85\n\t"
      "v_mfma_f32_16x16x32_bf16 a[16:19], v[96:99], v[8:11], a[16:19]\n\t"
      "v_cvt_pk_bf16_f32 v106, v84, v85\n\t"
      "v_and_b32 v234, s89, v196\n\t"
      "v_mfma_f32_16x16x32_bf16 a[32:35], v[96:99], v[16:19], a[32:35]\n\t"
      "s_waitcnt vmcnt(7)\n\t"
      "v_exp_f32 v86, v86\n\t"
      "v_mfma_f32_16x16x32_bf16 a[32:35], v[100:103], v[20:23], a[32:35]\n\t"
      "v_cmp_ne_u64 vcc, v[166:167], v[164:165]\n\t"
      "v_and_b32 v235, s89, v200\n\t"
      "v_mfma_f32_16x16x32_bf16 a[48:51], v[100:103], v[28:31], a[48:51]\n\t"
      "s_waitcnt lgkmcnt(0)\n\t"
      "v_exp_f32 v87, v87\n\t"
      "v_mfma_f32_16x16x32_bf16 a[48:51], v[96:99], v[24:27], a[48:51]\n\t"
      "v_cvt_pk_bf16_f32 v107, v86, v87\n\t"
      "v_mul_f32 v232, v175, v232\n\t"
      "s_cbranch_vccnz .Lspin_loop\n\t"
      ".Lback_loop:\n\t"
      "v_mfma_f32_16x16x32_bf16 a[64:67], v[96:99], v[32:35], a[64:67]\n\t"
      "s_add_u32 m0, s85, 0\n\t"
      "v_exp_f32 v88, v88\n\t"
      "v_mfma_f32_16x16x32_bf16 a[64:67], v[100:103], v[36:39], a[64:67]\n\t"
      "buffer_load_dwordx4 v152, s[36:39], s24 offen offset:0 sc1 lds\n\t"
      "v_mfma_f32_16x16x32_bf16 a[80:83], v[100:103], v[44:47], a[80:83]\n\t"
      "v_exp_f32 v89, v89\n\t"
      "v_mfma_f32_16x16x32_bf16 a[80:83], v[96:99], v[40:43], a[80:83]\n\t"
      "v_cvt_pk_bf16_f32 v108, v88, v89\n\t"
      "v_mul_f32 v233, v183, v233\n\t"
      "v_mfma_f32_16x16x32_bf16 a[96:99], v[96:99], v[48:51], a[96:99]\n\t"
      "s_add_u32 s94, s94, 256\n\t"
      "v_exp_f32 v90, v90\n\t"
      "v_mfma_f32_16x16x32_bf16 a[96:99], v[100:103], v[52:55], a[96:99]\n\t"
      "v_fma_f32 v232, -v183, v234, v232\n\t"
      "v_fmac_f32 v233, v175, v235\n\t"
      "v_mfma_f32_16x16x32_bf16 a[112:115], v[100:103], v[60:63], a[112:115]\n\t"
      "s_min_u32 s94, s94, 2048\n\t"
      "v_exp_f32 v91, v91\n\t"
      "v_mfma_f32_16x16x32_bf16 a[112:115], v[96:99], v[56:59], a[112:115]\n\t"
      "v_cvt_pk_bf16_f32 v109, v90, v91\n\t"
      "v_cvt_pk_bf16_f32 v238, v228, v232\n\t"
      "v_mfma_f32_16x16x32_bf16 v[128:131], v[96:99], v[248:251], v[128:131]\n\t"
      "v_exp_f32 v92, v92\n\t"
      "v_mfma_f32_16x16x32_bf16 v[128:131], v[100:103], v[248:251], v[128:131]\n\t"
      "buffer_load_dwordx4 v152, s[36:39], s24 offen offset:1024 sc1 lds\n\t"
      "v_mfma_f32_16x16x32_bf16 v[64:67], a[192:195], a[160:163], v[120:123]\n\t"
      "v_exp_f32 v93, v93\n\t"
      "v_mfma_f32_16x16x32_bf16 v[64:67], a[196:199], a[164:167], v[64:67]\n\t"
      "v_cvt_pk_bf16_f32 v110, v92, v93\n\t"
      "v_cvt_pk_bf16_f32 v242, v229, v233\n\t"
      "v_mfma_f32_16x16x32_bf16 v[64:67], a[200:203], a[168:171], v[64:67]\n\t"
      "v_exp_f32 v94, v94\n\t"
      "v_mfma_f32_16x16x32_bf16 v[64:67], a[204:207], a[172:175], v[64:67]\n\t"
      "v_cvt_f32_i32 v176, v176\n\t"
      "v_cvt_f32_i32 v184, v184\n\t"
      "v_mfma_f32_16x16x32_bf16 v[68:71], a[220:223], a[172:175], v[120:123]\n\t"
      "v_exp_f32 v95, v95\n\t"
      "v_mfma_f32_16x16x32_bf16 v[68:71], a[216:219], a[168:171], v[68:71]\n\t"
      "v_cvt_pk_bf16_f32 v111, v94, v95\n\t"
      "v_fma_f32 v176, v176, v224, v226\n\t"
      "v_mfma_f32_16x16x32_bf16 v[68:71], a[212:215], a[164:167], v[68:71]\n\t"
      "buffer_load_dwordx4 v152, s[36:39], s24 offen offset:2048 sc1 lds\n\t"
      "v_mfma_f32_16x16x32_bf16 v[68:71], a[208:211], a[160:163], v[68:71]\n\t"
      "v_fma_f32 v184, v184, v225, v227\n\t"
      "v_lshlrev_b32 v228, 16, v189\n\t"
      "v_mfma_f32_16x16x32_bf16 v[72:75], a[224:227], a[160:163], v[120:123]\n\t"
      "v_exp_f32 v64, v64\n\t"
      "v_mfma_f32_16x16x32_bf16 v[72:75], a[228:231], a[164:167], v[72:75]\n\t"
      "v_lshlrev_b32 v229, 16, v193\n\t"
      "v_lshlrev_b32 v230, 16, v197\n\t"
      "v_mfma_f32_16x16x32_bf16 v[72:75], a[232:235], a[168:171], v[72:75]\n\t"
      "v_exp_f32 v65, v65\n\t"
      "v_mfma_f32_16x16x32_bf16 v[72:75], a[236:239], a[172:175], v[72:75]\n\t"
      "v_cvt_pk_bf16_f32 v96, v64, v65\n\t"
      "v_lshlrev_b32 v231, 16, v201\n\t"
      "v_mul_f32 v228, v176, v228\n\t"
      "v_mfma_f32_16x16x32_bf16 v[76:79], a[252:255], a[172:175], v[120:123]\n\t"
      "v_exp_f32 v66, v66\n\t"
      "v_mfma_f32_16x16x32_bf16 v[76:79], a[248:251], a[168:171], v[76:79]\n\t"
      "v_mul_f32 v229, v184, v229\n\t"
      "v_fma_f32 v228, -v184, v230, v228\n\t"
      "v_mfma_f32_16x16x32_bf16 v[76:79], a[244:247], a[164:167], v[76:79]\n\t"
      "v_exp_f32 v67, v67\n\t"
      "v_mfma_f32_16x16x32_bf16 v[76:79], a[240:243], a[160:163], v[76:79]\n\t"
      "buffer_load_dwordx4 v152, s[36:39], s24 offen offset:3072 sc1 lds\n\t"
      "v_mfma_f32_16x16x32_bf16 a[4:7], v[104:107], v[0:3], a[4:7]\n\t"
      "s_add_u32 s24, s24, 4096\n\t"
      "v_exp_f32 v68, v68\n\t"
      "v_mfma_f32_16x16x32_bf16 a[4:7], v[108:111], v[4:7], a[4:7]\n\t"
      "v_cvt_pk_bf16_f32 v97, v66, v67\n\t"
      "v_fmac_f32 v229, v176, v231\n\t"
      "v_mfma_f32_16x16x32_bf16 a[20:23], v[108:111], v[12:15], a[20:23]\n\t"
      "s_add_u32 m0, s85, 4096\n\t"
      "v_exp_f32 v69, v69\n\t"
      "v_mfma_f32_16x16x32_bf16 a[20:23], v[104:107], v[8:11], a[20:23]\n\t"
      "v_cvt_pk_bf16_f32 v98, v68, v69\n\t"
      "v_cvt_f32_i32 v177, v177\n\t"
      "v_mfma_f32_16x16x32_bf16 a[36:39], v[104:107], v[16:19], a[36:39]\n\t"
      "v_exp_f32 v70, v70\n\t"
      "v_mfma_f32_16x16x32_bf16 a[36:39], v[108:111], v[20:23], a[36:39]\n\t"
      "v_cvt_f32_i32 v185, v185\n\t"
      "v_fma_f32 v177, v177, v224, v226\n\t"
      "v_mfma_f32_16x16x32_bf16 a[52:55], v[108:111], v[28:31], a[52:55]\n\t"
      "v_exp_f32 v71, v71\n\t"
      "v_mfma_f32_16x16x32_bf16 a[52:55], v[104:107], v[24:27], a[52:55]\n\t"
      "buffer_load_dwordx4 v152, s[36:39], s24 offen offset:0 sc1 lds\n\t"
      "v_mfma_f32_16x16x32_bf16 a[68:71], v[104:107], v[32:35], a[68:71]\n\t"
      "v_exp_f32 v72, v72\n\t"
      "v_mfma_f32_16x16x32_bf16 a[68:71], v[108:111], v[36:39], a[68:71]\n\t"
      "v_cvt_pk_bf16_f32 v99, v70, v71\n\t"
      "v_fma_f32 v185, v185, v225, v227\n\t"
      "v_mfma_f32_16x16x32_bf16 a[84:87], v[108:111], v[44:47], a[84:87]\n\t"
      "v_exp_f32 v73, v73\n\t"
      "v_mfma_f32_16x16x32_bf16 a[84:87], v[104:107], v[40:43], a[84:87]\n\t"
      "v_cvt_pk_bf16_f32 v100, v72, v73\n\t"
      "v_and_b32 v232, s89, v189\n\t"
      "v_mfma_f32_16x16x32_bf16 a[100:103], v[104:107], v[48:51], a[100:103]\n\t"
      "v_exp_f32 v74, v74\n\t"
      "v_mfma_f32_16x16x32_bf16 a[100:103], v[108:111], v[52:55], a[100:103]\n\t"
      "v_and_b32 v233, s89, v193\n\t"
      "v_and_b32 v234, s89, v197\n\t"
      "v_mfma_f32_16x16x32_bf16 a[116:119], v[108:111], v[60:63], a[116:119]\n\t"
      "v_exp_f32 v75, v75\n\t"
      "v_mfma_f32_16x16x32_bf16 a[116:119], v[104:107], v[56:59], a[116:119]\n\t"
      "buffer_load_dwordx4 v152, s[36:39], s24 offen offset:1024 sc1 lds\n\t"
      "v_mfma_f32_16x16x32_bf16 v[132:135], v[104:107], v[248:251], v[132:135]\n\t"
      "s_waitcnt vmcnt(10)\n\t"
      "v_exp_f32 v76, v76\n\t"
      "v_mfma_f32_16x16x32_bf16 v[132:135], v[108:111], v[248:251], v[132:135]\n\t"
      "s_barrier\n\t"
      "v_cvt_pk_bf16_f32 v101, v74, v75\n\t"
      "v_and_b32 v235, s89, v201\n\t"
      "v_mfma_f32_16x16x32_bf16 v[80:83], a[192:195], a[176:179], v[124:127]\n\t"
      "v_exp_f32 v77, v77\n\t"
      "v_mfma_f32_16x16x32_bf16 v[80:83], a[196:199], a[180:183], v[80:83]\n\t"
      "ds_read_b128 a[192:195], v136 offset:32768\n\t"
      "v_cvt_pk_bf16_f32 v102, v76, v77\n\t"
      "v_mfma_f32_16x16x32_bf16 v[80:83], a[200:203], a[184:187], v[80:83]\n\t"
      "v_exp_f32 v78, v78\n\t"
      "v_mfma_f32_16x16x32_bf16 v[80:83], a[204:207], a[188:191], v[80:83]\n\t"
      "ds_read_b128 a[196:199], v137 offset:32768\n\t"
      "v_mul_f32 v232, v177, v232\n\t"
      "v_mfma_f32_16x16x32_bf16 v[84:87], a[220:223], a[188:191], v[124:127]\n\t"
      "v_exp_f32 v79, v79\n\t"
      "v_mfma_f32_16x16x32_bf16 v[84:87], a[216:219], a[184:187], v[84:87]\n\t"
      "ds_read_b128 a[200:203], v138 offset:32768\n\t"
      "v_cvt_pk_bf16_f32 v103, v78, v79\n\t"
      "v_mfma_f32_16x16x32_bf16 v[84:87], a[212:215], a[180:183], v[84:87]\n\t"
      "buffer_load_dwordx4 v152, s[36:39], s24 offen offset:2048 sc1 lds\n\t"
      "v_mfma_f32_16x16x32_bf16 v[84:87], a[208:211], a[176:179], v[84:87]\n\t"
      "ds_read_b128 a[204:207], v139 offset:32768\n\t"
      "v_mul_f32 v233, v185, v233\n\t"
      "v_mfma_f32_16x16x32_bf16 v[88:91], a[224:227], a[176:179], v[124:127]\n\t"
      "v_exp_f32 v80, v80\n\t"
      "v_mfma_f32_16x16x32_bf16 v[88:91], a[228:231], a[180:183], v[88:91]\n\t"
      "ds_read_b128 a[208:211], v136 offset:36864\n\t"
      "v_fma_f32 v232, -v185, v234, v232\n\t"
      "v_mfma_f32_16x16x32_bf16 v[88:91], a[232:235], a[184:187], v[88:91]\n\t"
      "v_exp_f32 v81, v81\n\t"
      "v_mfma_f32_16x16x32_bf16 v[88:91], a[236:239], a[188:191], v[88:91]\n\t"
      "ds_read_b128 a[212:215], v137 offset:36864\n\t"
      "v_cvt_pk_bf16_f32 v104, v80, v81\n\t"
      "v_mfma_f32_16x16x32_bf16 v[92:95], a[252:255], a[188:191], v[124:127]\n\t"
      "v_exp_f32 v82, v82\n\t"
      "v_mfma_f32_16x16x32_bf16 v[92:95], a[248:251], a[184:187], v[92:95]\n\t"
      "ds_read_b128 a[216:219], v138 offset:36864\n\t"
      "v_fmac_f32 v233, v177, v235\n\t"
      "v_mfma_f32_16x16x32_bf16 v[92:95], a[244:247], a[180:183], v[92:95]\n\t"
      "v_exp_f32 v83, v83\n\t"
      "v_mfma_f32_16x16x32_bf16 v[92:95], a[240:243], a[176:179], v[92:95]\n\t"
      "ds_read_b128 a[220:223], v139 offset:36864\n\t"
      "v_cvt_pk_bf16_f32 v105, v82, v83\n\t"
      "v_mfma_f32_16x16x32_bf16 a[8:11], v[96:99], v[0:3], a[8:11]\n\t"
      "v_exp_f32 v84, v84\n\t"
      "v_mfma_f32_16x16x32_bf16 a[8:11], v[100:103], v[4:7], a[8:11]\n\t"
      "ds_read_b128 a[224:227], v136 offset:40960\n\t"
      "v_cvt_pk_bf16_f32 v239, v228, v232\n\t"
      "v_cvt_pk_bf16_f32 v243, v229, v233\n\t"
      "v_mfma_f32_16x16x32_bf16 a[24:27], v[100:103], v[12:15], a[24:27]\n\t"
      "buffer_store_dwordx4 v[236:239], v157, s[36:39], s93 offen sc1\n\t"
      "v_exp_f32 v85, v85\n\t"
      "v_mfma_f32_16x16x32_bf16 a[24:27], v[96:99], v[8:11], a[24:27]\n\t"
      "ds_read_b128 a[228:231], v137 offset:40960\n\t"
      "v_cvt_pk_bf16_f32 v106, v84, v85\n\t"
      "v_mfma_f32_16x16x32_bf16 a[40:43], v[96:99], v[16:19], a[40:43]\n\t"
      "buffer_store_dwordx4 v[240:243], v158, s[36:39], s93 offen sc1\n\t"
      "v_exp_f32 v86, v86\n\t"
      "v_mfma_f32_16x16x32_bf16 a[40:43], v[100:103], v[20:23], a[40:43]\n\t"
      "ds_read_b128 a[232:235], v138 offset:40960\n\t"
      "v_mfma_f32_16x16x32_bf16 a[56:59], v[100:103], v[28:31], a[56:59]\n\t"
      "s_waitcnt vmcnt(9)\n\t"
      "v_exp_f32 v87, v87\n\t"
      "v_mfma_f32_16x16x32_bf16 a[56:59], v[96:99], v[24:27], a[56:59]\n\t"
      "ds_read_b128 a[236:239], v139 offset:40960\n\t"
      "v_cvt_pk_bf16_f32 v107, v86, v87\n\t"
      "v_mfma_f32_16x16x32_bf16 a[72:75], v[96:99], v[32:35], a[72:75]\n\t"
      "v_exp_f32 v88, v88\n\t"
      "v_mfma_f32_16x16x32_bf16 a[72:75], v[100:103], v[36:39], a[72:75]\n\t"
      "ds_read_b128 a[240:243], v136 offset:45056\n\t"
      "v_lshlrev_b32 v222, 16, v222\n\t"
      "v_mfma_f32_16x16x32_bf16 a[88:91], v[100:103], v[44:47], a[88:91]\n\t"
      "v_exp_f32 v89, v89\n\t"
      "v_mfma_f32_16x16x32_bf16 a[88:91], v[96:99], v[40:43], a[88:91]\n\t"
      "ds_read_b128 a[244:247], v137 offset:45056\n\t"
      "v_cvt_pk_bf16_f32 v108, v88, v89\n\t"
      "v_mfma_f32_16x16x32_bf16 a[104:107], v[96:99], v[48:51], a[104:107]\n\t"
      "v_exp_f32 v90, v90\n\t"
      "v_mfma_f32_16x16x32_bf16 a[104:107], v[100:103], v[52:55], a[104:107]\n\t"
      "ds_read_b128 a[248:251], v138 offset:45056\n\t"
      "v_lshlrev_b32 v223, 16, v223\n\t"
      "v_mfma_f32_16x16x32_bf16 a[120:123], v[100:103], v[60:63], a[120:123]\n\t"
      "s_cmp_eq_u32 s86, 1\n\t"
      "v_exp_f32 v91, v91\n\t"
      "v_mfma_f32_16x16x32_bf16 a[120:123], v[96:99], v[56:59], a[120:123]\n\t"
      "ds_read_b128 a[252:255], v139 offset:45056\n\t"
      "v_cvt_pk_bf16_f32 v109, v90, v91\n\t"
      "v_mfma_f32_16x16x32_bf16 v[140:143], v[96:99], v[248:251], v[140:143]\n\t"
      "s_waitcnt lgkmcnt(12)\n\t"
      "v_exp_f32 v92, v92\n\t"
      "v_mfma_f32_16x16x32_bf16 v[140:143], v[100:103], v[248:251], v[140:143]\n\t"
      "buffer_load_dwordx4 v152, s[36:39], s24 offen offset:3072 sc1 lds\n\t"
      "s_cbranch_scc1 .Lfinal\n\t"
      "v_mfma_f32_16x16x32_bf16 v[64:67], a[192:195], a[128:131], v[112:115]\n\t"
      "s_add_u32 s24, s24, 126976\n\t"
      "v_exp_f32 v93, v93\n\t"
      "v_mfma_f32_16x16x32_bf16 v[64:67], a[196:199], a[132:135], v[64:67]\n\t"
      "v_cvt_pk_bf16_f32 v110, v92, v93\n\t"
      "v_cvt_f32_i32 v206, v206\n\t"
      "v_mfma_f32_16x16x32_bf16 v[64:67], a[200:203], a[136:139], v[64:67]\n\t"
      "s_waitcnt lgkmcnt(8)\n\t"
      "v_exp_f32 v94, v94\n\t"
      "v_mfma_f32_16x16x32_bf16 v[64:67], a[204:207], a[140:143], v[64:67]\n\t"
      "v_cvt_f32_i32 v207, v207\n\t"
      "v_cvt_f32_i32 v208, v208\n\t"
      "v_mfma_f32_16x16x32_bf16 v[68:71], a[220:223], a[140:143], v[112:115]\n\t"
      "buffer_load_dwordx4 v[170:173], v153, s[40:43], s90 offen offset:0\n\t"
      "v_exp_f32 v95, v95\n\t"
      "v_mfma_f32_16x16x32_bf16 v[68:71], a[216:219], a[136:139], v[68:71]\n\t"
      "s_waitcnt lgkmcnt(4)\n\t"
      "v_cvt_pk_bf16_f32 v111, v94, v95\n\t"
      "v_cvt_f32_i32 v209, v209\n\t"
      "v_mfma_f32_16x16x32_bf16 v[68:71], a[212:215], a[132:135], v[68:71]\n\t"
      "v_subrev_f32_dpp v206, v223, v206 row_newbcast:0 row_mask:0xf bank_mask:0xf\n\t"
      "v_subrev_f32_dpp v207, v223, v207 row_newbcast:0 row_mask:0xf bank_mask:0xf\n\t"
      "v_mfma_f32_16x16x32_bf16 v[68:71], a[208:211], a[128:131], v[68:71]\n\t"
      "v_subrev_f32_dpp v208, v223, v208 row_newbcast:1 row_mask:0xf bank_mask:0xf\n\t"
      "v_subrev_f32_dpp v209, v223, v209 row_newbcast:1 row_mask:0xf bank_mask:0xf\n\t"
      "v_mfma_f32_16x16x32_bf16 v[72:75], a[224:227], a[128:131], v[112:115]\n\t"
      "v_exp_f32 v64, v64\n\t"
      "v_mfma_f32_16x16x32_bf16 v[72:75], a[228:231], a[132:135], v[72:75]\n\t"
      "v_mul_f32_dpp v206, v222, v206 row_newbcast:0 row_mask:0xf bank_mask:0xf\n\t"
      "v_mul_f32_dpp v207, v222, v207 row_newbcast:0 row_mask:0xf bank_mask:0xf\n\t"
      "v_mfma_f32_16x16x32_bf16 v[72:75], a[232:235], a[136:139], v[72:75]\n\t"
      "s_waitcnt lgkmcnt(0)\n\t"
      "v_exp_f32 v65, v65\n\t"
      "v_mfma_f32_16x16x32_bf16 v[72:75], a[236:239], a[140:143], v[72:75]\n\t"
      "v_cvt_pk_bf16_f32 v96, v64, v65\n\t"
      "v_subrev_u32 v136, 0x10000, v136\n\t"
      "v_mfma_f32_16x16x32_bf16 v[76:79], a[252:255], a[140:143], v[112:115]\n\t"
      "buffer_load_dwordx4 v[174:177], v153, s[40:43], s90 offen offset:16\n\t"
      "v_exp_f32 v66, v66\n\t"
      "v_mfma_f32_16x16x32_bf16 v[76:79], a[248:251], a[136:139], v[76:79]\n\t"
      "v_subrev_u32 v137, 0x10000, v137\n\t"
      "v_subrev_u32 v138, 0x10000, v138\n\t"
      "v_mfma_f32_16x16x32_bf16 v[76:79], a[244:247], a[132:135], v[76:79]\n\t"
      "v_exp_f32 v67, v67\n\t"
      "v_mfma_f32_16x16x32_bf16 v[76:79], a[240:243], a[128:131], v[76:79]\n\t"
      "v_cvt_pk_bf16_f32 v97, v66, v67\n\t"
      "v_subrev_u32 v139, 0x10000, v139\n\t"
      "v_mfma_f32_16x16x32_bf16 a[12:15], v[104:107], v[0:3], a[12:15]\n\t"
      "v_exp_f32 v68, v68\n\t"
      "v_mfma_f32_16x16x32_bf16 a[12:15], v[108:111], v[4:7], a[12:15]\n\t"
      "ds_read_b128 v[0:3], v144 offset:49152\n\t"
      "v_mul_f32_dpp v208, v222, v208 row_newbcast:1 row_mask:0xf bank_mask:0xf\n\t"
      "v_mfma_f32_16x16x32_bf16 a[28:31], v[108:111], v[12:15], a[28:31]\n\t"
      "v_exp_f32 v69, v69\n\t"
      "v_mfma_f32_16x16x32_bf16 a[28:31], v[104:107], v[8:11], a[28:31]\n\t"
      "ds_read_b128 v[4:7], v144 offset:57344\n\t"
      "v_cvt_pk_bf16_f32 v98, v68, v69\n\t"
      "v_mfma_f32_16x16x32_bf16 a[44:47], v[104:107], v[16:19], a[44:47]\n\t"
      "v_exp_f32 v70, v70\n\t"
      "v_mfma_f32_16x16x32_bf16 a[44:47], v[108:111], v[20:23], a[44:47]\n\t"
      "ds_read_b128 v[8:11], v144 offset:50176\n\t"
      "v_mul_f32_dpp v209, v222, v209 row_newbcast:1 row_mask:0xf bank_mask:0xf\n\t"
      "v_cvt_pk_bf16_f32 v228, v206, v208\n\t"
      "v_mfma_f32_16x16x32_bf16 a[60:63], v[108:111], v[28:31], a[60:63]\n\t"
      "v_exp_f32 v71, v71\n\t"
      "v_mfma_f32_16x16x32_bf16 a[60:63], v[104:107], v[24:27], a[60:63]\n\t"
      "ds_read_b128 v[12:15], v144 offset:58368\n\t"
      "v_cvt_pk_bf16_f32 v99, v70, v71\n\t"
      "v_mfma_f32_16x16x32_bf16 a[76:79], v[104:107], v[32:35], a[76:79]\n\t"
      "v_exp_f32 v72, v72\n\t"
      "v_mfma_f32_16x16x32_bf16 a[76:79], v[108:111], v[36:39], a[76:79]\n\t"
      "ds_read_b128 v[16:19], v145 offset:49152\n\t"
      "v_cvt_pk_bf16_f32 v232, v207, v209\n\t"
      "v_mfma_f32_16x16x32_bf16 a[92:95], v[108:111], v[44:47], a[92:95]\n\t"
      "v_exp_f32 v73, v73\n\t"
      "v_mfma_f32_16x16x32_bf16 a[92:95], v[104:107], v[40:43], a[92:95]\n\t"
      "ds_read_b128 v[20:23], v145 offset:57344\n\t"
      "v_cvt_pk_bf16_f32 v100, v72, v73\n\t"
      "v_mfma_f32_16x16x32_bf16 a[108:111], v[104:107], v[48:51], a[108:111]\n\t"
      "v_exp_f32 v74, v74\n\t"
      "v_mfma_f32_16x16x32_bf16 a[108:111], v[108:111], v[52:55], a[108:111]\n\t"
      "ds_read_b128 v[24:27], v145 offset:50176\n\t"
      "v_cvt_f32_i32 v210, v210\n\t"
      "v_mfma_f32_16x16x32_bf16 a[124:127], v[108:111], v[60:63], a[124:127]\n\t"
      "v_exp_f32 v75, v75\n\t"
      "v_mfma_f32_16x16x32_bf16 a[124:127], v[104:107], v[56:59], a[124:127]\n\t"
      "ds_read_b128 v[28:31], v145 offset:58368\n\t"
      "v_cvt_pk_bf16_f32 v101, v74, v75\n\t"
      "v_mfma_f32_16x16x32_bf16 v[148:151], v[104:107], v[248:251], v[148:151]\n\t"
      "v_exp_f32 v76, v76\n\t"
      "v_mfma_f32_16x16x32_bf16 v[148:151], v[108:111], v[248:251], v[148:151]\n\t"
      "ds_read_b128 v[32:35], v146 offset:49152\n\t"
      "v_cvt_f32_i32 v211, v211\n\t"
      "v_mfma_f32_16x16x32_bf16 v[80:83], a[192:195], a[144:147], v[116:119]\n\t"
      "v_exp_f32 v77, v77\n\t"
      "v_mfma_f32_16x16x32_bf16 v[80:83], a[196:199], a[148:151], v[80:83]\n\t"
      "ds_read_b128 v[36:39], v146 offset:57344\n\t"
      "v_cvt_pk_bf16_f32 v102, v76, v77\n\t"
      "v_mfma_f32_16x16x32_bf16 v[80:83], a[200:203], a[152:155], v[80:83]\n\t"
      "v_exp_f32 v78, v78\n\t"
      "v_mfma_f32_16x16x32_bf16 v[80:83], a[204:207], a[156:159], v[80:83]\n\t"
      "ds_read_b128 v[40:43], v146 offset:50176\n\t"
      "v_cvt_f32_i32 v212, v212\n\t"
      "v_mfma_f32_16x16x32_bf16 v[84:87], a[220:223], a[156:159], v[116:119]\n\t"
      "v_exp_f32 v79, v79\n\t"
      "v_mfma_f32_16x16x32_bf16 v[84:87], a[216:219], a[152:155], v[84:87]\n\t"
      "ds_read_b128 v[44:47], v146 offset:58368\n\t"
      "v_cvt_pk_bf16_f32 v103, v78, v79\n\t"
      "v_mfma_f32_16x16x32_bf16 v[84:87], a[212:215], a[148:151], v[84:87]\n\t"
      "v_cvt_f32_i32 v213, v213\n\t"
      "v_subrev_f32_dpp v210, v223, v210 row_newbcast:2 row_mask:0xf bank_mask:0xf\n\t"
      "v_mfma_f32_16x16x32_bf16 v[84:87], a[208:211], a[144:147], v[84:87]\n\t"
      "ds_read_b128 v[48:51], v147 offset:49152\n\t"
      "v_subrev_f32_dpp v211, v223, v211 row_newbcast:2 row_mask:0xf bank_mask:0xf\n\t"
      "v_mfma_f32_16x16x32_bf16 v[88:91], a[224:227], a[144:147], v[116:119]\n\t"
      "v_exp_f32 v80, v80\n\t"
      "v_mfma_f32_16x16x32_bf16 v[88:91], a[228:231], a[148:151], v[88:91]\n\t"
      "ds_read_b128 v[52:55], v147 offset:57344\n\t"
      "v_subrev_f32_dpp v212, v223, v212 row_newbcast:3 row_mask:0xf bank_mask:0xf\n\t"
      "v_mfma_f32_16x16x32_bf16 v[88:91], a[232:235], a[152:155], v[88:91]\n\t"
      "v_exp_f32 v81, v81\n\t"
      "v_mfma_f32_16x16x32_bf16 v[88:91], a[236:239], a[156:159], v[88:91]\n\t"
      "ds_read_b128 v[56:59], v147 offset:50176\n\t"
      "v_cvt_pk_bf16_f32 v104, v80, v81\n\t"
      "v_mfma_f32_16x16x32_bf16 v[92:95], a[252:255], a[156:159], v[116:119]\n\t"
      "v_exp_f32 v82, v82\n\t"
      "v_mfma_f32_16x16x32_bf16 v[92:95], a[248:251], a[152:155], v[92:95]\n\t"
      "ds_read_b128 v[60:63], v147 offset:58368\n\t"
      "v_subrev_f32_dpp v213, v223, v213 row_newbcast:3 row_mask:0xf bank_mask:0xf\n\t"
      "v_mfma_f32_16x16x32_bf16 v[92:95], a[244:247], a[148:151], v[92:95]\n\t"
      "v_exp_f32 v83, v83\n\t"
      "v_mfma_f32_16x16x32_bf16 v[92:95], a[240:243], a[144:147], v[92:95]\n\t"
      "s_waitcnt lgkmcnt(8)\n\t"
      "v_cvt_pk_bf16_f32 v105, v82, v83\n\t"
      "v_mul_f32_dpp v210, v222, v210 row_newbcast:2 row_mask:0xf bank_mask:0xf\n\t"
      "v_mfma_f32_16x16x32_bf16 a[0:3], v[96:99], v[0:3], a[0:3]\n\t"
      "v_exp_f32 v84, v84\n\t"
      "v_mfma_f32_16x16x32_bf16 a[0:3], v[100:103], v[4:7], a[0:3]\n\t"
      "v_mul_f32_dpp v211, v222, v211 row_newbcast:2 row_mask:0xf bank_mask:0xf\n\t"
      "v_mul_f32_dpp v212, v222, v212 row_newbcast:3 row_mask:0xf bank_mask:0xf\n\t"
      "v_mfma_f32_16x16x32_bf16 a[16:19], v[100:103], v[12:15], a[16:19]\n\t"
      "v_exp_f32 v85, v85\n\t"
      "v_mfma_f32_16x16x32_bf16 a[16:19], v[96:99], v[8:11], a[16:19]\n\t"
      "v_cvt_pk_bf16_f32 v106, v84, v85\n\t"
      "v_mul_f32_dpp v213, v222, v213 row_newbcast:3 row_mask:0xf bank_mask:0xf\n\t"
      "v_mfma_f32_16x16x32_bf16 a[32:35], v[96:99], v[16:19], a[32:35]\n\t"
      "v_exp_f32 v86, v86\n\t"
      "v_mfma_f32_16x16x32_bf16 a[32:35], v[100:103], v[20:23], a[32:35]\n\t"
      "v_cvt_pk_bf16_f32 v229, v210, v212\n\t"
      "v_cvt_pk_bf16_f32 v233, v211, v213\n\t"
      "v_mfma_f32_16x16x32_bf16 a[48:51], v[100:103], v[28:31], a[48:51]\n\t"
      "s_waitcnt lgkmcnt(0)\n\t"
      "v_exp_f32 v87, v87\n\t"
      "v_mfma_f32_16x16x32_bf16 a[48:51], v[96:99], v[24:27], a[48:51]\n\t"
      "v_cvt_pk_bf16_f32 v107, v86, v87\n\t"
      "v_cvt_f32_i32 v214, v214\n\t"
      "v_mfma_f32_16x16x32_bf16 a[64:67], v[96:99], v[32:35], a[64:67]\n\t"
      "s_add_u32 m0, s85, 32768\n\t"
      "v_exp_f32 v88, v88\n\t"
      "v_mfma_f32_16x16x32_bf16 a[64:67], v[100:103], v[36:39], a[64:67]\n\t"
      "buffer_load_dwordx4 v152, s[36:39], s25 offen offset:0 sc1 lds\n\t"
      "v_mfma_f32_16x16x32_bf16 a[80:83], v[100:103], v[44:47], a[80:83]\n\t"
      "v_exp_f32 v89, v89\n\t"
      "v_mfma_f32_16x16x32_bf16 a[80:83], v[96:99], v[40:43], a[80:83]\n\t"
      "v_cvt_pk_bf16_f32 v108, v88, v89\n\t"
      "v_subrev_u32 v144, 0x10000, v144\n\t"
      "v_cvt_f32_i32 v215, v215\n\t"
      "v_mfma_f32_16x16x32_bf16 a[96:99], v[96:99], v[48:51], a[96:99]\n\t"
      "v_exp_f32 v90, v90\n\t"
      "v_mfma_f32_16x16x32_bf16 a[96:99], v[100:103], v[52:55], a[96:99]\n\t"
      "v_subrev_u32 v145, 0x10000, v145\n\t"
      "v_subrev_u32 v146, 0x10000, v146\n\t"
      "v_mfma_f32_16x16x32_bf16 a[112:115], v[100:103], v[60:63], a[112:115]\n\t"
      "v_exp_f32 v91, v91\n\t"
      "v_mfma_f32_16x16x32_bf16 a[112:115], v[96:99], v[56:59], a[112:115]\n\t"
      "v_cvt_pk_bf16_f32 v109, v90, v91\n\t"
      "v_subrev_u32 v147, 0x10000, v147\n\t"
      "v_mfma_f32_16x16x32_bf16 v[128:131], v[96:99], v[248:251], v[128:131]\n\t"
      "v_exp_f32 v92, v92\n\t"
      "v_mfma_f32_16x16x32_bf16 v[128:131], v[100:103], v[248:251], v[128:131]\n\t"
      "buffer_load_dwordx4 v152, s[36:39], s25 offen offset:1024 sc1 lds\n\t"
      "v_mfma_f32_16x16x32_bf16 v[64:67], a[192:195], a[160:163], v[120:123]\n\t"
      "v_exp_f32 v93, v93\n\t"
      "v_mfma_f32_16x16x32_bf16 v[64:67], a[196:199], a[164:167], v[64:67]\n\t"
      "v_cvt_pk_bf16_f32 v110, v92, v93\n\t"
      "v_cvt_f32_i32 v216, v216\n\t"
      "v_mfma_f32_16x16x32_bf16 v[64:67], a[200:203], a[168:171], v[64:67]\n\t"
      "v_exp_f32 v94, v94\n\t"
      "v_mfma_f32_16x16x32_bf16 v[64:67], a[204:207], a[172:175], v[64:67]\n\t"
      "v_cvt_f32_i32 v217, v217\n\t"
      "v_subrev_f32_dpp v214, v223, v214 row_newbcast:4 row_mask:0xf bank_mask:0xf\n\t"
      "v_mfma_f32_16x16x32_bf16 v[68:71], a[220:223], a[172:175], v[120:123]\n\t"
      "v_exp_f32 v95, v95\n\t"
      "v_mfma_f32_16x16x32_bf16 v[68:71], a[216:219], a[168:171], v[68:71]\n\t"
      "v_cvt_pk_bf16_f32 v111, v94, v95\n\t"
      "v_subrev_f32_dpp v215, v223, v215 row_newbcast:4 row_mask:0xf bank_mask:0xf\n\t"
      "v_mfma_f32_16x16x32_bf16 v[68:71], a[212:215], a[164:167], v[68:71]\n\t"
      "buffer_load_dwordx4 v152, s[36:39], s25 offen offset:2048 sc1 lds\n\t"
      "v_mfma_f32_16x16x32_bf16 v[68:71], a[208:211], a[160:163], v[68:71]\n\t"
      "v_subrev_f32_dpp v216, v223, v216 row_newbcast:5 row_mask:0xf bank_mask:0xf\n\t"
      "v_subrev_f32_dpp v217, v223, v217 row_newbcast:5 row_mask:0xf bank_mask:0xf\n\t"
      "v_mfma_f32_16x16x32_bf16 v[72:75], a[224:227], a[160:163], v[120:123]\n\t"
      "v_exp_f32 v64, v64\n\t"
      "v_mfma_f32_16x16x32_bf16 v[72:75], a[228:231], a[164:167], v[72:75]\n\t"
      "v_mul_f32_dpp v214, v222, v214 row_newbcast:4 row_mask:0xf bank_mask:0xf\n\t"
      "v_mul_f32_dpp v215, v222, v215 row_newbcast:4 row_mask:0xf bank_mask:0xf\n\t"
      "v_mfma_f32_16x16x32_bf16 v[72:75], a[232:235], a[168:171], v[72:75]\n\t"
      "v_exp_f32 v65, v65\n\t"
      "v_mfma_f32_16x16x32_bf16 v[72:75], a[236:239], a[172:175], v[72:75]\n\t"
      "v_cvt_pk_bf16_f32 v96, v64, v65\n\t"
      "v_mul_f32_dpp v216, v222, v216 row_newbcast:5 row_mask:0xf bank_mask:0xf\n\t"
      "v_mfma_f32_16x16x32_bf16 v[76:79], a[252:255], a[172:175], v[120:123]\n\t"
      "v_exp_f32 v66, v66\n\t"
      "v_mfma_f32_16x16x32_bf16 v[76:79], a[248:251], a[168:171], v[76:79]\n\t"
      "v_mul_f32_dpp v217, v222, v217 row_newbcast:5 row_mask:0xf bank_mask:0xf\n\t"
      "v_cvt_pk_bf16_f32 v230, v214, v216\n\t"
      "v_mfma_f32_16x16x32_bf16 v[76:79], a[244:247], a[164:167], v[76:79]\n\t"
      "v_exp_f32 v67, v67\n\t"
      "v_mfma_f32_16x16x32_bf16 v[76:79], a[240:243], a[160:163], v[76:79]\n\t"
      "buffer_load_dwordx4 v152, s[36:39], s25 offen offset:3072 sc1 lds\n\t"
      "v_mfma_f32_16x16x32_bf16 a[4:7], v[104:107], v[0:3], a[4:7]\n\t"
      "s_add_u32 s25, s25, 4096\n\t"
      "v_exp_f32 v68, v68\n\t"
      "v_mfma_f32_16x16x32_bf16 a[4:7], v[108:111], v[4:7], a[4:7]\n\t"
      "v_cvt_pk_bf16_f32 v97, v66, v67\n\t"
      "v_cvt_pk_bf16_f32 v234, v215, v217\n\t"
      "v_mfma_f32_16x16x32_bf16 a[20:23], v[108:111], v[12:15], a[20:23]\n\t"
      "s_add_u32 m0, s85, 36864\n\t"
      "v_exp_f32 v69, v69\n\t"
      "v_mfma_f32_16x16x32_bf16 a[20:23], v[104:107], v[8:11], a[20:23]\n\t"
      "v_cvt_pk_bf16_f32 v98, v68, v69\n\t"
      "v_cvt_f32_i32 v218, v218\n\t"
      "v_mfma_f32_16x16x32_bf16 a[36:39], v[104:107], v[16:19], a[36:39]\n\t"
      "v_exp_f32 v70, v70\n\t"
      "v_mfma_f32_16x16x32_bf16 a[36:39], v[108:111], v[20:23], a[36:39]\n\t"
      "v_cvt_f32_i32 v219, v219\n\t"
      "v_cvt_f32_i32 v220, v220\n\t"
      "v_mfma_f32_16x16x32_bf16 a[52:55], v[108:111], v[28:31], a[52:55]\n\t"
      "v_exp_f32 v71, v71\n\t"
      "v_mfma_f32_16x16x32_bf16 a[52:55], v[104:107], v[24:27], a[52:55]\n\t"
      "buffer_load_dwordx4 v152, s[36:39], s25 offen offset:0 sc1 lds\n\t"
      "v_mfma_f32_16x16x32_bf16 a[68:71], v[104:107], v[32:35], a[68:71]\n\t"
      "v_exp_f32 v72, v72\n\t"
      "v_mfma_f32_16x16x32_bf16 a[68:71], v[108:111], v[36:39], a[68:71]\n\t"
      "v_cvt_pk_bf16_f32 v99, v70, v71\n\t"
      "v_cvt_f32_i32 v221, v221\n\t"
      "v_mfma_f32_16x16x32_bf16 a[84:87], v[108:111], v[44:47], a[84:87]\n\t"
      "v_exp_f32 v73, v73\n\t"
      "v_mfma_f32_16x16x32_bf16 a[84:87], v[104:107], v[40:43], a[84:87]\n\t"
      "v_cvt_pk_bf16_f32 v100, v72, v73\n\t"
      "v_subrev_f32_dpp v218, v223, v218 row_newbcast:6 row_mask:0xf bank_mask:0xf\n\t"
      "v_mfma_f32_16x16x32_bf16 a[100:103], v[104:107], v[48:51], a[100:103]\n\t"
      "v_exp_f32 v74, v74\n\t"
      "v_mfma_f32_16x16x32_bf16 a[100:103], v[108:111], v[52:55], a[100:103]\n\t"
      "v_subrev_f32_dpp v219, v223, v219 row_newbcast:6 row_mask:0xf bank_mask:0xf\n\t"
      "v_subrev_f32_dpp v220, v223, v220 row_newbcast:7 row_mask:0xf bank_mask:0xf\n\t"
      "v_mfma_f32_16x16x32_bf16 a[116:119], v[108:111], v[60:63], a[116:119]\n\t"
      "v_exp_f32 v75, v75\n\t"
      "v_mfma_f32_16x16x32_bf16 a[116:119], v[104:107], v[56:59], a[116:119]\n\t"
      "buffer_load_dwordx4 v152, s[36:39], s25 offen offset:1024 sc1 lds\n\t"
      "v_mfma_f32_16x16x32_bf16 v[132:135], v[104:107], v[248:251], v[132:135]\n\t"
      "s_waitcnt vmcnt(8)\n\t"
      "v_exp_f32 v76, v76\n\t"
      "v_mfma_f32_16x16x32_bf16 v[132:135], v[108:111], v[248:251], v[132:135]\n\t"
      "s_barrier\n\t"
      "v_cvt_pk_bf16_f32 v101, v74, v75\n\t"
      "v_subrev_f32_dpp v221, v223, v221 row_newbcast:7 row_mask:0xf bank_mask:0xf\n\t"
      "v_mul_f32_dpp v218, v222, v218 row_newbcast:6 row_mask:0xf bank_mask:0xf\n\t"
      "v_mfma_f32_16x16x32_bf16 v[80:83], a[192:195], a[176:179], v[124:127]\n\t"
      "v_exp_f32 v77, v77\n\t"
      "v_mfma_f32_16x16x32_bf16 v[80:83], a[196:199], a[180:183], v[80:83]\n\t"
      "ds_read_b128 a[192:195], v136 offset:0\n\t"
      "v_cvt_pk_bf16_f32 v102, v76, v77\n\t"
      "v_mfma_f32_16x16x32_bf16 v[80:83], a[200:203], a[184:187], v[80:83]\n\t"
      "v_exp_f32 v78, v78\n\t"
      "v_mfma_f32_16x16x32_bf16 v[80:83], a[204:207], a[188:191], v[80:83]\n\t"
      "ds_read_b128 a[196:199], v137 offset:0\n\t"
      "v_mul_f32_dpp v219, v222, v219 row_newbcast:6 row_mask:0xf bank_mask:0xf\n\t"
      "v_mfma_f32_16x16x32_bf16 v[84:87], a[220:223], a[188:191], v[124:127]\n\t"
      "v_exp_f32 v79, v79\n\t"
      "v_mfma_f32_16x16x32_bf16 v[84:87], a[216:219], a[184:187], v[84:87]\n\t"
      "ds_read_b128 a[200:203], v138 offset:0\n\t"
      "v_cvt_pk_bf16_f32 v103, v78, v79\n\t"
      "v_mfma_f32_16x16x32_bf16 v[84:87], a[212:215], a[180:183], v[84:87]\n\t"
      "buffer_load_dwordx4 v152, s[36:39], s25 offen offset:2048 sc1 lds\n\t"
      "v_mfma_f32_16x16x32_bf16 v[84:87], a[208:211], a[176:179], v[84:87]\n\t"
      "ds_read_b128 a[204:207], v139 offset:0\n\t"
      "v_mul_f32_dpp v220, v222, v220 row_newbcast:7 row_mask:0xf bank_mask:0xf\n\t"
      "v_mfma_f32_16x16x32_bf16 v[88:91], a[224:227], a[176:179], v[124:127]\n\t"
      "v_exp_f32 v80, v80\n\t"
      "v_mfma_f32_16x16x32_bf16 v[88:91], a[228:231], a[180:183], v[88:91]\n\t"
      "ds_read_b128 a[208:211], v136 offset:4096\n\t"
      "v_mul_f32_dpp v221, v222, v221 row_newbcast:7 row_mask:0xf bank_mask:0xf\n\t"
      "v_mfma_f32_16x16x32_bf16 v[88:91], a[232:235], a[184:187], v[88:91]\n\t"
      "buffer_load_dwordx4 v[178:181], v153, s[40:43], s90 offen offset:256\n\t"
      "v_exp_f32 v81, v81\n\t"
      "v_mfma_f32_16x16x32_bf16 v[88:91], a[236:239], a[188:191], v[88:91]\n\t"
      "ds_read_b128 a[212:215], v137 offset:4096\n\t"
      "v_cvt_pk_bf16_f32 v104, v80, v81\n\t"
      "v_mfma_f32_16x16x32_bf16 v[92:95], a[252:255], a[188:191], v[124:127]\n\t"
      "v_exp_f32 v82, v82\n\t"
      "v_mfma_f32_16x16x32_bf16 v[92:95], a[248:251], a[184:187], v[92:95]\n\t"
      "ds_read_b128 a[216:219], v138 offset:4096\n\t"
      "v_cvt_pk_bf16_f32 v231, v218, v220\n\t"
      "v_mfma_f32_16x16x32_bf16 v[92:95], a[244:247], a[180:183], v[92:95]\n\t"
      "v_exp_f32 v83, v83\n\t"
      "v_mfma_f32_16x16x32_bf16 v[92:95], a[240:243], a[176:179], v[92:95]\n\t"
      "ds_read_b128 a[220:223], v139 offset:4096\n\t"
      "v_cvt_pk_bf16_f32 v105, v82, v83\n\t"
      "v_mfma_f32_16x16x32_bf16 a[8:11], v[96:99], v[0:3], a[8:11]\n\t"
      "v_exp_f32 v84, v84\n\t"
      "v_mfma_f32_16x16x32_bf16 a[8:11], v[100:103], v[4:7], a[8:11]\n\t"
      "ds_read_b128 a[224:227], v136 offset:8192\n\t"
      "v_cvt_pk_bf16_f32 v235, v219, v221\n\t"
      "v_mfma_f32_16x16x32_bf16 a[24:27], v[100:103], v[12:15], a[24:27]\n\t"
      "buffer_load_dwordx4 v[182:185], v153, s[40:43], s90 offen offset:272\n\t"
      "v_exp_f32 v85, v85\n\t"
      "v_mfma_f32_16x16x32_bf16 a[24:27], v[96:99], v[8:11], a[24:27]\n\t"
      "ds_read_b128 a[228:231], v137 offset:8192\n\t"
      "v_cvt_pk_bf16_f32 v106, v84, v85\n\t"
      "v_mfma_f32_16x16x32_bf16 a[40:43], v[96:99], v[16:19], a[40:43]\n\t"
      "buffer_store_dwordx4 v[228:231], v161, s[36:39], s93 offen sc1\n\t"
      "v_exp_f32 v86, v86\n\t"
      "v_mfma_f32_16x16x32_bf16 a[40:43], v[100:103], v[20:23], a[40:43]\n\t"
      "ds_read_b128 a[232:235], v138 offset:8192\n\t"
      "v_mfma_f32_16x16x32_bf16 a[56:59], v[100:103], v[28:31], a[56:59]\n\t"
      "buffer_store_dwordx4 v[232:235], v161, s[36:39], s93 offen offset:1024 sc1\n\t"
      "v_exp_f32 v87, v87\n\t"
      "v_mfma_f32_16x16x32_bf16 a[56:59], v[96:99], v[24:27], a[56:59]\n\t"
      "ds_read_b128 a[236:239], v139 offset:8192\n\t"
      "v_cvt_pk_bf16_f32 v107, v86, v87\n\t"
      "v_mfma_f32_16x16x32_bf16 a[72:75], v[96:99], v[32:35], a[72:75]\n\t"
      "s_add_u32 s93, s93, 131072\n\t"
      "v_exp_f32 v88, v88\n\t"
      "v_mfma_f32_16x16x32_bf16 a[72:75], v[100:103], v[36:39], a[72:75]\n\t"
      "ds_read_b128 a[240:243], v136 offset:12288\n\t"
      "v_mfma_f32_16x16x32_bf16 a[88:91], v[100:103], v[44:47], a[88:91]\n\t"
      "s_sub_u32 s86, s86, 1\n\t"
      "v_exp_f32 v89, v89\n\t"
      "v_mfma_f32_16x16x32_bf16 a[88:91], v[96:99], v[40:43], a[88:91]\n\t"
      "ds_read_b128 a[244:247], v137 offset:12288\n\t"
      "v_cvt_pk_bf16_f32 v108, v88, v89\n\t"
      "v_mfma_f32_16x16x32_bf16 a[104:107], v[96:99], v[48:51], a[104:107]\n\t"
      "v_exp_f32 v90, v90\n\t"
      "v_mfma_f32_16x16x32_bf16 a[104:107], v[100:103], v[52:55], a[104:107]\n\t"
      "ds_read_b128 a[248:251], v138 offset:12288\n\t"
      "v_mfma_f32_16x16x32_bf16 a[120:123], v[100:103], v[60:63], a[120:123]\n\t"
      "s_cmp_lg_u32 s86, 0\n\t"
      "v_exp_f32 v91, v91\n\t"
      "v_mfma_f32_16x16x32_bf16 a[120:123], v[96:99], v[56:59], a[120:123]\n\t"
      "ds_read_b128 a[252:255], v139 offset:12288\n\t"
      "v_cvt_pk_bf16_f32 v109, v90, v91\n\t"
      "v_mfma_f32_16x16x32_bf16 v[140:143], v[96:99], v[248:251], v[140:143]\n\t"
      "s_waitcnt lgkmcnt(12)\n\t"
      "v_exp_f32 v92, v92\n\t"
      "v_mfma_f32_16x16x32_bf16 v[140:143], v[100:103], v[248:251], v[140:143]\n\t"
      "buffer_load_dwordx4 v152, s[36:39], s25 offen offset:3072 sc1 lds\n\t"
      "s_cbranch_scc1 .Lattn_loop\n\t"
      ".Lfinal:\n\t"
      "s_waitcnt vmcnt(1)\n\t"
      "v_mfma_f32_16x16x32_bf16 v[64:67], a[192:195], a[128:131], v[112:115]\n\t"
      "v_exp_f32 v93, v93\n\t"
      "v_mfma_f32_16x16x32_bf16 v[64:67], a[196:199], a[132:135], v[64:67]\n\t"
      "v_cvt_pk_bf16_f32 v110, v92, v93\n\t"
      "v_mfma_f32_16x16x32_bf16 v[64:67], a[200:203], a[136:139], v[64:67]\n\t"
      "s_waitcnt lgkmcnt(8)\n\t"
      "v_exp_f32 v94, v94\n\t"
      "v_mfma_f32_16x16x32_bf16 v[64:67], a[204:207], a[140:143], v[64:67]\n\t"
      "v_mfma_f32_16x16x32_bf16 v[68:71], a[220:223], a[140:143], v[112:115]\n\t"
      "v_exp_f32 v95, v95\n\t"
      "v_mfma_f32_16x16x32_bf16 v[68:71], a[216:219], a[136:139], v[68:71]\n\t"
      "s_waitcnt lgkmcnt(4)\n\t"
      "v_cvt_pk_bf16_f32 v111, v94, v95\n\t"
      "v_mfma_f32_16x16x32_bf16 v[68:71], a[212:215], a[132:135], v[68:71]\n\t"
      "v_mfma_f32_16x16x32_bf16 v[68:71], a[208:211], a[128:131], v[68:71]\n\t"
      "v_mfma_f32_16x16x32_bf16 v[72:75], a[224:227], a[128:131], v[112:115]\n\t"
      "v_exp_f32 v64, v64\n\t"
      "v_mfma_f32_16x16x32_bf16 v[72:75], a[228:231], a[132:135], v[72:75]\n\t"
      "v_mfma_f32_16x16x32_bf16 v[72:75], a[232:235], a[136:139], v[72:75]\n\t"
      "s_waitcnt lgkmcnt(0)\n\t"
      "v_exp_f32 v65, v65\n\t"
      "v_mfma_f32_16x16x32_bf16 v[72:75], a[236:239], a[140:143], v[72:75]\n\t"
      "v_cvt_pk_bf16_f32 v96, v64, v65\n\t"
      "v_mfma_f32_16x16x32_bf16 v[76:79], a[252:255], a[140:143], v[112:115]\n\t"
      "v_exp_f32 v66, v66\n\t"
      "v_mfma_f32_16x16x32_bf16 v[76:79], a[248:251], a[136:139], v[76:79]\n\t"
      "v_mfma_f32_16x16x32_bf16 v[76:79], a[244:247], a[132:135], v[76:79]\n\t"
      "v_exp_f32 v67, v67\n\t"
      "v_mfma_f32_16x16x32_bf16 v[76:79], a[240:243], a[128:131], v[76:79]\n\t"
      "v_cvt_pk_bf16_f32 v97, v66, v67\n\t"
      "v_mfma_f32_16x16x32_bf16 a[12:15], v[104:107], v[0:3], a[12:15]\n\t"
      "v_exp_f32 v68, v68\n\t"
      "v_mfma_f32_16x16x32_bf16 a[12:15], v[108:111], v[4:7], a[12:15]\n\t"
      "ds_read_b128 v[0:3], v144 offset:49152\n\t"
      "v_mfma_f32_16x16x32_bf16 a[28:31], v[108:111], v[12:15], a[28:31]\n\t"
      "v_exp_f32 v69, v69\n\t"
      "v_mfma_f32_16x16x32_bf16 a[28:31], v[104:107], v[8:11], a[28:31]\n\t"
      "ds_read_b128 v[4:7], v144 offset:57344\n\t"
      "v_cvt_pk_bf16_f32 v98, v68, v69\n\t"
      "v_mfma_f32_16x16x32_bf16 a[44:47], v[104:107], v[16:19], a[44:47]\n\t"
      "v_exp_f32 v70, v70\n\t"
      "v_mfma_f32_16x16x32_bf16 a[44:47], v[108:111], v[20:23], a[44:47]\n\t"
      "ds_read_b128 v[8:11], v144 offset:50176\n\t"
      "v_mfma_f32_16x16x32_bf16 a[60:63], v[108:111], v[28:31], a[60:63]\n\t"
      "v_exp_f32 v71, v71\n\t"
      "v_mfma_f32_16x16x32_bf16 a[60:63], v[104:107], v[24:27], a[60:63]\n\t"
      "ds_read_b128 v[12:15], v144 offset:58368\n\t"
      "v_cvt_pk_bf16_f32 v99, v70, v71\n\t"
      "v_mfma_f32_16x16x32_bf16 a[76:79], v[104:107], v[32:35], a[76:79]\n\t"
      "v_exp_f32 v72, v72\n\t"
      "v_mfma_f32_16x16x32_bf16 a[76:79], v[108:111], v[36:39], a[76:79]\n\t"
      "ds_read_b128 v[16:19], v145 offset:49152\n\t"
      "v_mfma_f32_16x16x32_bf16 a[92:95], v[108:111], v[44:47], a[92:95]\n\t"
      "v_exp_f32 v73, v73\n\t"
      "v_mfma_f32_16x16x32_bf16 a[92:95], v[104:107], v[40:43], a[92:95]\n\t"
      "ds_read_b128 v[20:23], v145 offset:57344\n\t"
      "v_cvt_pk_bf16_f32 v100, v72, v73\n\t"
      "v_mfma_f32_16x16x32_bf16 a[108:111], v[104:107], v[48:51], a[108:111]\n\t"
      "v_exp_f32 v74, v74\n\t"
      "v_mfma_f32_16x16x32_bf16 a[108:111], v[108:111], v[52:55], a[108:111]\n\t"
      "ds_read_b128 v[24:27], v145 offset:50176\n\t"
      "v_mfma_f32_16x16x32_bf16 a[124:127], v[108:111], v[60:63], a[124:127]\n\t"
      "v_exp_f32 v75, v75\n\t"
      "v_mfma_f32_16x16x32_bf16 a[124:127], v[104:107], v[56:59], a[124:127]\n\t"
      "ds_read_b128 v[28:31], v145 offset:58368\n\t"
      "v_cvt_pk_bf16_f32 v101, v74, v75\n\t"
      "v_mfma_f32_16x16x32_bf16 v[148:151], v[104:107], v[248:251], v[148:151]\n\t"
      "v_exp_f32 v76, v76\n\t"
      "v_mfma_f32_16x16x32_bf16 v[148:151], v[108:111], v[248:251], v[148:151]\n\t"
      "ds_read_b128 v[32:35], v146 offset:49152\n\t"
      "v_mfma_f32_16x16x32_bf16 v[80:83], a[192:195], a[144:147], v[116:119]\n\t"
      "v_exp_f32 v77, v77\n\t"
      "v_mfma_f32_16x16x32_bf16 v[80:83], a[196:199], a[148:151], v[80:83]\n\t"
      "ds_read_b128 v[36:39], v146 offset:57344\n\t"
      "v_cvt_pk_bf16_f32 v102, v76, v77\n\t"
      "v_mfma_f32_16x16x32_bf16 v[80:83], a[200:203], a[152:155], v[80:83]\n\t"
      "v_exp_f32 v78, v78\n\t"
      "v_mfma_f32_16x16x32_bf16 v[80:83], a[204:207], a[156:159], v[80:83]\n\t"
      "ds_read_b128 v[40:43], v146 offset:50176\n\t"
      "v_mfma_f32_16x16x32_bf16 v[84:87], a[220:223], a[156:159], v[116:119]\n\t"
      "v_exp_f32 v79, v79\n\t"
      "v_mfma_f32_16x16x32_bf16 v[84:87], a[216:219], a[152:155], v[84:87]\n\t"
      "ds_read_b128 v[44:47], v146 offset:58368\n\t"
      "v_cvt_pk_bf16_f32 v103, v78, v79\n\t"
      "v_mfma_f32_16x16x32_bf16 v[84:87], a[212:215], a[148:151], v[84:87]\n\t"
      "v_mfma_f32_16x16x32_bf16 v[84:87], a[208:211], a[144:147], v[84:87]\n\t"
      "ds_read_b128 v[48:51], v147 offset:49152\n\t"
      "v_mfma_f32_16x16x32_bf16 v[88:91], a[224:227], a[144:147], v[116:119]\n\t"
      "v_exp_f32 v80, v80\n\t"
      "v_mfma_f32_16x16x32_bf16 v[88:91], a[228:231], a[148:151], v[88:91]\n\t"
      "ds_read_b128 v[52:55], v147 offset:57344\n\t"
      "v_mfma_f32_16x16x32_bf16 v[88:91], a[232:235], a[152:155], v[88:91]\n\t"
      "v_exp_f32 v81, v81\n\t"
      "v_mfma_f32_16x16x32_bf16 v[88:91], a[236:239], a[156:159], v[88:91]\n\t"
      "ds_read_b128 v[56:59], v147 offset:50176\n\t"
      "v_cvt_pk_bf16_f32 v104, v80, v81\n\t"
      "v_mfma_f32_16x16x32_bf16 v[92:95], a[252:255], a[156:159], v[116:119]\n\t"
      "v_exp_f32 v82, v82\n\t"
      "v_mfma_f32_16x16x32_bf16 v[92:95], a[248:251], a[152:155], v[92:95]\n\t"
      "ds_read_b128 v[60:63], v147 offset:58368\n\t"
      "v_mfma_f32_16x16x32_bf16 v[92:95], a[244:247], a[148:151], v[92:95]\n\t"
      "v_exp_f32 v83, v83\n\t"
      "v_mfma_f32_16x16x32_bf16 v[92:95], a[240:243], a[144:147], v[92:95]\n\t"
      "s_waitcnt lgkmcnt(8)\n\t"
      "v_cvt_pk_bf16_f32 v105, v82, v83\n\t"
      "v_mfma_f32_16x16x32_bf16 a[0:3], v[96:99], v[0:3], a[0:3]\n\t"
      "v_exp_f32 v84, v84\n\t"
      "v_mfma_f32_16x16x32_bf16 a[0:3], v[100:103], v[4:7], a[0:3]\n\t"
      "v_mfma_f32_16x16x32_bf16 a[16:19], v[100:103], v[12:15], a[16:19]\n\t"
      "v_exp_f32 v85, v85\n\t"
      "v_mfma_f32_16x16x32_bf16 a[16:19], v[96:99], v[8:11], a[16:19]\n\t"
      "v_cvt_pk_bf16_f32 v106, v84, v85\n\t"
      "v_mfma_f32_16x16x32_bf16 a[32:35], v[96:99], v[16:19], a[32:35]\n\t"
      "v_exp_f32 v86, v86\n\t"
      "v_mfma_f32_16x16x32_bf16 a[32:35], v[100:103], v[20:23], a[32:35]\n\t"
      "v_mfma_f32_16x16x32_bf16 a[48:51], v[100:103], v[28:31], a[48:51]\n\t"
      "s_waitcnt lgkmcnt(0)\n\t"
      "v_exp_f32 v87, v87\n\t"
      "v_mfma_f32_16x16x32_bf16 a[48:51], v[96:99], v[24:27], a[48:51]\n\t"
      "v_cvt_pk_bf16_f32 v107, v86, v87\n\t"
      "v_mfma_f32_16x16x32_bf16 a[64:67], v[96:99], v[32:35], a[64:67]\n\t"
      "v_exp_f32 v88, v88\n\t"
      "v_mfma_f32_16x16x32_bf16 a[64:67], v[100:103], v[36:39], a[64:67]\n\t"
      "v_mov_b32 v224, %3\n\t"
      "v_mfma_f32_16x16x32_bf16 a[80:83], v[100:103], v[44:47], a[80:83]\n\t"
      "v_exp_f32 v89, v89\n\t"
      "v_add_u32 v224, 147456, v224\n\t"
      "v_mfma_f32_16x16x32_bf16 a[80:83], v[96:99], v[40:43], a[80:83]\n\t"
      "v_cvt_pk_bf16_f32 v108, v88, v89\n\t"
      "v_mbcnt_lo_u32_b32 v240, -1, 0\n\t"
      "v_mfma_f32_16x16x32_bf16 a[96:99], v[96:99], v[48:51], a[96:99]\n\t"
      "v_exp_f32 v90, v90\n\t"
      "v_mbcnt_hi_u32_b32 v240, -1, v240\n\t"
      "v_mfma_f32_16x16x32_bf16 a[96:99], v[100:103], v[52:55], a[96:99]\n\t"
      "v_lshrrev_b32 v241, 4, v240\n\t"
      "v_mfma_f32_16x16x32_bf16 a[112:115], v[100:103], v[60:63], a[112:115]\n\t"
      "v_exp_f32 v91, v91\n\t"
      "v_and_b32 v242, 15, v240\n\t"
      "v_mfma_f32_16x16x32_bf16 a[112:115], v[96:99], v[56:59], a[112:115]\n\t"
      "v_cvt_pk_bf16_f32 v109, v90, v91\n\t"
      "v_lshlrev_b32 v242, 4, v242\n\t"
      "v_mfma_f32_16x16x32_bf16 v[128:131], v[96:99], v[248:251], v[128:131]\n\t"
      "v_exp_f32 v92, v92\n\t"
      "v_lshl_add_u32 v242, v241, 10, v242\n\t"
      "v_mfma_f32_16x16x32_bf16 v[128:131], v[100:103], v[248:251], v[128:131]\n\t"
      "s_mov_b64 s[72:73], s[98:99]\n\t"
      "v_mfma_f32_16x16x32_bf16 v[64:67], a[192:195], a[160:163], v[120:123]\n\t"
      "v_exp_f32 v93, v93\n\t"
      "s_mov_b32 s74, 16384\n\t"
      "v_mfma_f32_16x16x32_bf16 v[64:67], a[196:199], a[164:167], v[64:67]\n\t"
      "v_cvt_pk_bf16_f32 v110, v92, v93\n\t"
      "s_mov_b32 s75, 0x20000\n\t"
      "v_mfma_f32_16x16x32_bf16 v[64:67], a[200:203], a[168:171], v[64:67]\n\t"
      "v_exp_f32 v94, v94\n\t"
      "v_mov_b32_dpp v128, v128 row_newbcast:0 row_mask:0xf bank_mask:0xf\n\t"
      "v_mov_b32_dpp v129, v129 row_newbcast:0 row_mask:0xf bank_mask:0xf\n\t"
      "v_mfma_f32_16x16x32_bf16 v[64:67], a[204:207], a[172:175], v[64:67]\n\t"
      "v_mov_b32_dpp v130, v130 row_newbcast:0 row_mask:0xf bank_mask:0xf\n\t"
      "v_mov_b32_dpp v131, v131 row_newbcast:0 row_mask:0xf bank_mask:0xf\n\t"
      "v_rcp_f32 v170, v128\n\t"
      "v_mfma_f32_16x16x32_bf16 v[68:71], a[220:223], a[172:175], v[120:123]\n\t"
      "v_exp_f32 v95, v95\n\t"
      "v_rcp_f32 v172, v129\n\t"
      "v_rcp_f32 v174, v130\n\t"
      "v_rcp_f32 v176, v131\n\t"
      "v_mfma_f32_16x16x32_bf16 v[68:71], a[216:219], a[168:171], v[68:71]\n\t"
      "v_cvt_pk_bf16_f32 v111, v94, v95\n\t"
      "s_add_u32 s88, s97, 0\n\t"
      "s_and_b32 s88, s88, 0x3fff\n\t"
      "v_accvgpr_read_b32 v178, a0\n\t"
      "v_mfma_f32_16x16x32_bf16 v[68:71], a[212:215], a[164:167], v[68:71]\n\t"
      "v_accvgpr_read_b32 v179, a16\n\t"
      "v_accvgpr_read_b32 v180, a32\n\t"
      "v_mfma_f32_16x16x32_bf16 v[68:71], a[208:211], a[160:163], v[68:71]\n\t"
      "v_accvgpr_read_b32 v181, a48\n\t"
      "v_accvgpr_read_b32 v182, a64\n\t"
      "v_accvgpr_read_b32 v183, a80\n\t"
      "v_mfma_f32_16x16x32_bf16 v[72:75], a[224:227], a[160:163], v[120:123]\n\t"
      "v_exp_f32 v64, v64\n\t"
      "v_accvgpr_read_b32 v184, a96\n\t"
      "v_accvgpr_read_b32 v185, a112\n\t"
      "v_mul_f32 v178, v178, v170\n\t"
      "v_mfma_f32_16x16x32_bf16 v[72:75], a[228:231], a[164:167], v[72:75]\n\t"
      "v_mul_f32 v179, v179, v170\n\t"
      "v_mul_f32 v180, v180, v170\n\t"
      "v_mul_f32 v181, v181, v170\n\t"
      "v_mfma_f32_16x16x32_bf16 v[72:75], a[232:235], a[168:171], v[72:75]\n\t"
      "v_exp_f32 v65, v65\n\t"
      "v_mul_f32 v182, v182, v170\n\t"
      "v_mul_f32 v183, v183, v170\n\t"
      "v_mfma_f32_16x16x32_bf16 v[72:75], a[236:239], a[172:175], v[72:75]\n\t"
      "v_cvt_pk_bf16_f32 v96, v64, v65\n\t"
      "v_mul_f32 v184, v184, v170\n\t"
      "v_mul_f32 v185, v185, v170\n\t"
      "v_cvt_pk_bf16_f32 v186, v178, v179\n\t"
      "v_mfma_f32_16x16x32_bf16 v[76:79], a[252:255], a[172:175], v[120:123]\n\t"
      "v_exp_f32 v66, v66\n\t"
      "v_cvt_pk_bf16_f32 v187, v180, v181\n\t"
      "v_cvt_pk_bf16_f32 v188, v182, v183\n\t"
      "v_cvt_pk_bf16_f32 v189, v184, v185\n\t"
      "v_mfma_f32_16x16x32_bf16 v[76:79], a[248:251], a[168:171], v[76:79]\n\t"
      "buffer_store_dwordx4 v[186:189], v242, s[72:75], s88 offen offset:0 sc1\n\t"
      "v_accvgpr_read_b32 v190, a1\n\t"
      "v_accvgpr_read_b32 v191, a17\n\t"
      "v_mfma_f32_16x16x32_bf16 v[76:79], a[244:247], a[164:167], v[76:79]\n\t"
      "v_exp_f32 v67, v67\n\t"
      "v_accvgpr_read_b32 v192, a33\n\t"
      "v_accvgpr_read_b32 v193, a49\n\t"
      "v_mfma_f32_16x16x32_bf16 v[76:79], a[240:243], a[160:163], v[76:79]\n\t"
      "v_accvgpr_read_b32 v194, a65\n\t"
      "v_accvgpr_read_b32 v195, a81\n\t"
      "v_accvgpr_read_b32 v196, a97\n\t"
      "v_mfma_f32_16x16x32_bf16 a[4:7], v[104:107], v[0:3], a[4:7]\n\t"
      "v_exp_f32 v68, v68\n\t"
      "v_accvgpr_read_b32 v197, a113\n\t"
      "v_mul_f32 v190, v190, v172\n\t"
      "v_mul_f32 v191, v191, v172\n\t"
      "v_mfma_f32_16x16x32_bf16 a[4:7], v[108:111], v[4:7], a[4:7]\n\t"
      "v_cvt_pk_bf16_f32 v97, v66, v67\n\t"
      "v_mul_f32 v192, v192, v172\n\t"
      "v_mul_f32 v193, v193, v172\n\t"
      "v_mul_f32 v194, v194, v172\n\t"
      "v_mfma_f32_16x16x32_bf16 a[20:23], v[108:111], v[12:15], a[20:23]\n\t"
      "v_exp_f32 v69, v69\n\t"
      "v_mul_f32 v195, v195, v172\n\t"
      "v_mul_f32 v196, v196, v172\n\t"
      "v_mul_f32 v197, v197, v172\n\t"
      "v_mfma_f32_16x16x32_bf16 a[20:23], v[104:107], v[8:11], a[20:23]\n\t"
      "v_cvt_pk_bf16_f32 v98, v68, v69\n\t"
      "v_cvt_pk_bf16_f32 v198, v190, v191\n\t"
      "v_cvt_pk_bf16_f32 v199, v192, v193\n\t"
      "v_mfma_f32_16x16x32_bf16 a[36:39], v[104:107], v[16:19], a[36:39]\n\t"
      "v_exp_f32 v70, v70\n\t"
      "v_cvt_pk_bf16_f32 v200, v194, v195\n\t"
      "v_cvt_pk_bf16_f32 v201, v196, v197\n\t"
      "buffer_store_dwordx4 v[198:201], v242, s[72:75], s88 offen offset:256 sc1\n\t"
      "v_mfma_f32_16x16x32_bf16 a[36:39], v[108:111], v[20:23], a[36:39]\n\t"
      "v_accvgpr_read_b32 v202, a2\n\t"
      "v_accvgpr_read_b32 v203, a18\n\t"
      "v_accvgpr_read_b32 v204, a34\n\t"
      "v_mfma_f32_16x16x32_bf16 a[52:55], v[108:111], v[28:31], a[52:55]\n\t"
      "v_exp_f32 v71, v71\n\t"
      "v_accvgpr_read_b32 v205, a50\n\t"
      "v_accvgpr_read_b32 v206, a66\n\t"
      "v_accvgpr_read_b32 v207, a82\n\t"
      "v_mfma_f32_16x16x32_bf16 a[52:55], v[104:107], v[24:27], a[52:55]\n\t"
      "v_accvgpr_read_b32 v208, a98\n\t"
      "v_accvgpr_read_b32 v209, a114\n\t"
      "v_mfma_f32_16x16x32_bf16 a[68:71], v[104:107], v[32:35], a[68:71]\n\t"
      "v_exp_f32 v72, v72\n\t"
      "v_mul_f32 v202, v202, v174\n\t"
      "v_mul_f32 v203, v203, v174\n\t"
      "v_mul_f32 v204, v204, v174\n\t"
      "v_mfma_f32_16x16x32_bf16 a[68:71], v[108:111], v[36:39], a[68:71]\n\t"
      "v_cvt_pk_bf16_f32 v99, v70, v71\n\t"
      "v_mul_f32 v205, v205, v174\n\t"
      "v_mul_f32 v206, v206, v174\n\t"
      "v_mul_f32 v207, v207, v174\n\t"
      "v_mfma_f32_16x16x32_bf16 a[84:87], v[108:111], v[44:47], a[84:87]\n\t"
      "v_exp_f32 v73, v73\n\t"
      "v_mul_f32 v208, v208, v174\n\t"
      "v_mul_f32 v209, v209, v174\n\t"
      "v_cvt_pk_bf16_f32 v210, v202, v203\n\t"
      "v_mfma_f32_16x16x32_bf16 a[84:87], v[104:107], v[40:43], a[84:87]\n\t"
      "v_cvt_pk_bf16_f32 v100, v72, v73\n\t"
      "v_cvt_pk_bf16_f32 v211, v204, v205\n\t"
      "v_cvt_pk_bf16_f32 v212, v206, v207\n\t"
      "v_mfma_f32_16x16x32_bf16 a[100:103], v[104:107], v[48:51], a[100:103]\n\t"
      "v_exp_f32 v74, v74\n\t"
      "v_cvt_pk_bf16_f32 v213, v208, v209\n\t"
      "buffer_store_dwordx4 v[210:213], v242, s[72:75], s88 offen offset:512 sc1\n\t"
      "v_accvgpr_read_b32 v178, a3\n\t"
      "v_mfma_f32_16x16x32_bf16 a[100:103], v[108:111], v[52:55], a[100:103]\n\t"
      "v_accvgpr_read_b32 v179, a19\n\t"
      "v_accvgpr_read_b32 v180, a35\n\t"
      "v_accvgpr_read_b32 v181, a51\n\t"
      "v_mfma_f32_16x16x32_bf16 a[116:119], v[108:111], v[60:63], a[116:119]\n\t"
      "v_exp_f32 v75, v75\n\t"
      "v_accvgpr_read_b32 v182, a67\n\t"
      "v_accvgpr_read_b32 v183, a83\n\t"
      "v_accvgpr_read_b32 v184, a99\n\t"
      "v_mfma_f32_16x16x32_bf16 a[116:119], v[104:107], v[56:59], a[116:119]\n\t"
      "v_accvgpr_read_b32 v185, a115\n\t"
      "v_mul_f32 v178, v178, v176\n\t"
      "v_mfma_f32_16x16x32_bf16 v[132:135], v[104:107], v[248:251], v[132:135]\n\t"
      "v_exp_f32 v76, v76\n\t"
      "v_mul_f32 v179, v179, v176\n\t"
      "v_mul_f32 v180, v180, v176\n\t"
      "v_mul_f32 v181, v181, v176\n\t"
      "v_mfma_f32_16x16x32_bf16 v[132:135], v[108:111], v[248:251], v[132:135]\n\t"
      "v_cvt_pk_bf16_f32 v101, v74, v75\n\t"
      "v_mul_f32 v182, v182, v176\n\t"
      "v_mul_f32 v183, v183, v176\n\t"
      "v_mul_f32 v184, v184, v176\n\t"
      "v_mfma_f32_16x16x32_bf16 v[80:83], a[192:195], a[176:179], v[124:127]\n\t"
      "v_exp_f32 v77, v77\n\t"
      "v_mul_f32 v185, v185, v176\n\t"
      "v_cvt_pk_bf16_f32 v186, v178, v179\n\t"
      "v_cvt_pk_bf16_f32 v187, v180, v181\n\t"
      "v_mfma_f32_16x16x32_bf16 v[80:83], a[196:199], a[180:183], v[80:83]\n\t"
      "v_cvt_pk_bf16_f32 v102, v76, v77\n\t"
      "v_cvt_pk_bf16_f32 v188, v182, v183\n\t"
      "v_cvt_pk_bf16_f32 v189, v184, v185\n\t"
      "buffer_store_dwordx4 v[186:189], v242, s[72:75], s88 offen offset:768 sc1\n\t"
      "v_mfma_f32_16x16x32_bf16 v[80:83], a[200:203], a[184:187], v[80:83]\n\t"
      "v_exp_f32 v78, v78\n\t"
      "v_mov_b32_dpp v132, v132 row_newbcast:0 row_mask:0xf bank_mask:0xf\n\t"
      "v_mov_b32_dpp v133, v133 row_newbcast:0 row_mask:0xf bank_mask:0xf\n\t"
      "v_mfma_f32_16x16x32_bf16 v[80:83], a[204:207], a[188:191], v[80:83]\n\t"
      "v_mov_b32_dpp v134, v134 row_newbcast:0 row_mask:0xf bank_mask:0xf\n\t"
      "v_mov_b32_dpp v135, v135 row_newbcast:0 row_mask:0xf bank_mask:0xf\n\t"
      "v_rcp_f32 v170, v132\n\t"
      "v_mfma_f32_16x16x32_bf16 v[84:87], a[220:223], a[188:191], v[124:127]\n\t"
      "v_exp_f32 v79, v79\n\t"
      "v_rcp_f32 v172, v133\n\t"
      "v_rcp_f32 v174, v134\n\t"
      "v_rcp_f32 v176, v135\n\t"
      "v_mfma_f32_16x16x32_bf16 v[84:87], a[216:219], a[184:187], v[84:87]\n\t"
      "v_cvt_pk_bf16_f32 v103, v78, v79\n\t"
      "s_add_u32 s88, s97, 4096\n\t"
      "s_and_b32 s88, s88, 0x3fff\n\t"
      "v_accvgpr_read_b32 v190, a4\n\t"
      "v_mfma_f32_16x16x32_bf16 v[84:87], a[212:215], a[180:183], v[84:87]\n\t"
      "v_accvgpr_read_b32 v191, a20\n\t"
      "v_accvgpr_read_b32 v192, a36\n\t"
      "v_accvgpr_read_b32 v193, a52\n\t"
      "v_mfma_f32_16x16x32_bf16 v[84:87], a[208:211], a[176:179], v[84:87]\n\t"
      "v_accvgpr_read_b32 v194, a68\n\t"
      "v_accvgpr_read_b32 v195, a84\n\t"
      "v_accvgpr_read_b32 v196, a100\n\t"
      "v_mfma_f32_16x16x32_bf16 v[88:91], a[224:227], a[176:179], v[124:127]\n\t"
      "v_exp_f32 v80, v80\n\t"
      "v_accvgpr_read_b32 v197, a116\n\t"
      "v_mul_f32 v190, v190, v170\n\t"
      "v_mul_f32 v191, v191, v170\n\t"
      "v_mfma_f32_16x16x32_bf16 v[88:91], a[228:231], a[180:183], v[88:91]\n\t"
      "v_mul_f32 v192, v192, v170\n\t"
      "v_mul_f32 v193, v193, v170\n\t"
      "v_mul_f32 v194, v194, v170\n\t"
      "v_mfma_f32_16x16x32_bf16 v[88:91], a[232:235], a[184:187], v[88:91]\n\t"
      "v_exp_f32 v81, v81\n\t"
      "v_mul_f32 v195, v195, v170\n\t"
      "v_mul_f32 v196, v196, v170\n\t"
      "v_mul_f32 v197, v197, v170\n\t"
      "v_mfma_f32_16x16x32_bf16 v[88:91], a[236:239], a[188:191], v[88:91]\n\t"
      "v_cvt_pk_bf16_f32 v104, v80, v81\n\t"
      "v_cvt_pk_bf16_f32 v198, v190, v191\n\t"
      "v_cvt_pk_bf16_f32 v199, v192, v193\n\t"
      "v_cvt_pk_bf16_f32 v200, v194, v195\n\t"
      "v_mfma_f32_16x16x32_bf16 v[92:95], a[252:255], a[188:191], v[124:127]\n\t"
      "v_exp_f32 v82, v82\n\t"
      "v_cvt_pk_bf16_f32 v201, v196, v197\n\t"
      "buffer_store_dwordx4 v[198:201], v242, s[72:75], s88 offen offset:0 sc1\n\t"
      "v_accvgpr_read_b32 v202, a5\n\t"
      "v_mfma_f32_16x16x32_bf16 v[92:95], a[248:251], a[184:187], v[92:95]\n\t"
      "v_accvgpr_read_b32 v203, a21\n\t"
      "v_accvgpr_read_b32 v204, a37\n\t"
      "v_accvgpr_read_b32 v205, a53\n\t"
      "v_mfma_f32_16x16x32_bf16 v[92:95], a[244:247], a[180:183], v[92:95]\n\t"
      "v_exp_f32 v83, v83\n\t"
      "v_accvgpr_read_b32 v206, a69\n\t"
      "v_accvgpr_read_b32 v207, a85\n\t"
      "v_accvgpr_read_b32 v208, a101\n\t"
      "v_mfma_f32_16x16x32_bf16 v[92:95], a[240:243], a[176:179], v[92:95]\n\t"
      "v_cvt_pk_bf16_f32 v105, v82, v83\n\t"
      "v_accvgpr_read_b32 v209, a117\n\t"
      "v_mul_f32 v202, v202, v172\n\t"
      "v_mul_f32 v203, v203, v172\n\t"
      "v_mfma_f32_16x16x32_bf16 a[8:11], v[96:99], v[0:3], a[8:11]\n\t"
      "v_exp_f32 v84, v84\n\t"
      "v_mul_f32 v204, v204, v172\n\t"
      "v_mul_f32 v205, v205, v172\n\t"
      "v_mul_f32 v206, v206, v172\n\t"
      "v_mfma_f32_16x16x32_bf16 a[8:11], v[100:103], v[4:7], a[8:11]\n\t"
      "v_mul_f32 v207, v207, v172\n\t"
      "v_mul_f32 v208, v208, v172\n\t"
      "v_mul_f32 v209, v209, v172\n\t"
      "v_mfma_f32_16x16x32_bf16 a[24:27], v[100:103], v[12:15], a[24:27]\n\t"
      "v_exp_f32 v85, v85\n\t"
      "v_cvt_pk_bf16_f32 v210, v202, v203\n\t"
      "v_cvt_pk_bf16_f32 v211, v204, v205\n\t"
      "v_mfma_f32_16x16x32_bf16 a[24:27], v[96:99], v[8:11], a[24:27]\n\t"
      "v_cvt_pk_bf16_f32 v106, v84, v85\n\t"
      "v_cvt_pk_bf16_f32 v212, v206, v207\n\t"
      "v_cvt_pk_bf16_f32 v213, v208, v209\n\t"
      "buffer_store_dwordx4 v[210:213], v242, s[72:75], s88 offen offset:256 sc1\n\t"
      "v_mfma_f32_16x16x32_bf16 a[40:43], v[96:99], v[16:19], a[40:43]\n\t"
      "v_exp_f32 v86, v86\n\t"
      "v_accvgpr_read_b32 v178, a6\n\t"
      "v_accvgpr_read_b32 v179, a22\n\t"
      "v_accvgpr_read_b32 v180, a38\n\t"
      "v_mfma_f32_16x16x32_bf16 a[40:43], v[100:103], v[20:23], a[40:43]\n\t"
      "v_accvgpr_read_b32 v181, a54\n\t"
      "v_accvgpr_read_b32 v182, a70\n\t"
      "v_accvgpr_read_b32 v183, a86\n\t"
      "v_mfma_f32_16x16x32_bf16 a[56:59], v[100:103], v[28:31], a[56:59]\n\t"
      "v_exp_f32 v87, v87\n\t"
      "v_accvgpr_read_b32 v184, a102\n\t"
      "v_accvgpr_read_b32 v185, a118\n\t"
      "v_mul_f32 v178, v178, v174\n\t"
      "v_mfma_f32_16x16x32_bf16 a[56:59], v[96:99], v[24:27], a[56:59]\n\t"
      "v_cvt_pk_bf16_f32 v107, v86, v87\n\t"
      "v_mul_f32 v179, v179, v174\n\t"
      "v_mul_f32 v180, v180, v174\n\t"
      "v_mul_f32 v181, v181, v174\n\t"
      "v_mfma_f32_16x16x32_bf16 a[72:75], v[96:99], v[32:35], a[72:75]\n\t"
      "v_exp_f32 v88, v88\n\t"
      "v_mul_f32 v182, v182, v174\n\t"
      "v_mul_f32 v183, v183, v174\n\t"
      "v_mul_f32 v184, v184, v174\n\t"
      "v_mfma_f32_16x16x32_bf16 a[72:75], v[100:103], v[36:39], a[72:75]\n\t"
      "v_mul_f32 v185, v185, v174\n\t"
      "v_cvt_pk_bf16_f32 v186, v178, v179\n\t"
      "v_cvt_pk_bf16_f32 v187, v180, v181\n\t"
      "v_mfma_f32_16x16x32_bf16 a[88:91], v[100:103], v[44:47], a[88:91]\n\t"
      "v_exp_f32 v89, v89\n\t"
      "v_cvt_pk_bf16_f32 v188, v182, v183\n\t"
      "v_cvt_pk_bf16_f32 v189, v184, v185\n\t"
      "buffer_store_dwordx4 v[186:189], v242, s[72:75], s88 offen offset:512 sc1\n\t"
      "v_mfma_f32_16x16x32_bf16 a[88:91], v[96:99], v[40:43], a[88:91]\n\t"
      "v_cvt_pk_bf16_f32 v108, v88, v89\n\t"
      "v_accvgpr_read_b32 v190, a7\n\t"
      "v_accvgpr_read_b32 v191, a23\n\t"
      "v_accvgpr_read_b32 v192, a39\n\t"
      "v_mfma_f32_16x16x32_bf16 a[104:107], v[96:99], v[48:51], a[104:107]\n\t"
      "v_exp_f32 v90, v90\n\t"
      "v_accvgpr_read_b32 v193, a55\n\t"
      "v_accvgpr_read_b32 v194, a71\n\t"
      "v_accvgpr_read_b32 v195, a87\n\t"
      "v_mfma_f32_16x16x32_bf16 a[104:107], v[100:103], v[52:55], a[104:107]\n\t"
      "v_accvgpr_read_b32 v196, a103\n\t"
      "v_accvgpr_read_b32 v197, a119\n\t"
      "v_mul_f32 v190, v190, v176\n\t"
      "v_mfma_f32_16x16x32_bf16 a[120:123], v[100:103], v[60:63], a[120:123]\n\t"
      "v_exp_f32 v91, v91\n\t"
      "v_mul_f32 v191, v191, v176\n\t"
      "v_mul_f32 v192, v192, v176\n\t"
      "v_mul_f32 v193, v193, v176\n\t"
      "v_mfma_f32_16x16x32_bf16 a[120:123], v[96:99], v[56:59], a[120:123]\n\t"
      "v_cvt_pk_bf16_f32 v109, v90, v91\n\t"
      "v_mul_f32 v194, v194, v176\n\t"
      "v_mul_f32 v195, v195, v176\n\t"
      "v_mul_f32 v196, v196, v176\n\t"
      "v_mfma_f32_16x16x32_bf16 v[140:143], v[96:99], v[248:251], v[140:143]\n\t"
      "v_exp_f32 v92, v92\n\t"
      "v_mul_f32 v197, v197, v176\n\t"
      "v_cvt_pk_bf16_f32 v198, v190, v191\n\t"
      "v_cvt_pk_bf16_f32 v199, v192, v193\n\t"
      "v_mfma_f32_16x16x32_bf16 v[140:143], v[100:103], v[248:251], v[140:143]\n\t"
      "v_cvt_pk_bf16_f32 v200, v194, v195\n\t"
      "v_cvt_pk_bf16_f32 v201, v196, v197\n\t"
      "buffer_store_dwordx4 v[198:201], v242, s[72:75], s88 offen offset:768 sc1\n\t"
      "v_exp_f32 v93, v93\n\t"
      "v_exp_f32 v94, v94\n\t"
      "v_exp_f32 v95, v95\n\t"
      "v_cvt_pk_bf16_f32 v110, v92, v93\n\t"
      "v_cvt_pk_bf16_f32 v111, v94, v95\n\t"
      "v_mfma_f32_16x16x32_bf16 v[148:151], v[104:107], v[248:251], v[148:151]\n\t"
      "v_mov_b32_dpp v140, v140 row_newbcast:0 row_mask:0xf bank_mask:0xf\n\t"
      "v_mov_b32_dpp v141, v141 row_newbcast:0 row_mask:0xf bank_mask:0xf\n\t"
      "v_mov_b32_dpp v142, v142 row_newbcast:0 row_mask:0xf bank_mask:0xf\n\t"
      "v_mov_b32_dpp v143, v143 row_newbcast:0 row_mask:0xf bank_mask:0xf\n\t"
      "v_rcp_f32 v170, v140\n\t"
      "v_mfma_f32_16x16x32_bf16 v[148:151], v[108:111], v[248:251], v[148:151]\n\t"
      "v_rcp_f32 v172, v141\n\t"
      "v_rcp_f32 v174, v142\n\t"
      "v_rcp_f32 v176, v143\n\t"
      "s_add_u32 s88, s97, 8192\n\t"
      "s_and_b32 s88, s88, 0x3fff\n\t"
      "v_mfma_f32_16x16x32_bf16 a[12:15], v[104:107], v[0:3], a[12:15]\n\t"
      "v_accvgpr_read_b32 v202, a8\n\t"
      "v_accvgpr_read_b32 v203, a24\n\t"
      "v_accvgpr_read_b32 v204, a40\n\t"
      "v_accvgpr_read_b32 v205, a56\n\t"
      "v_accvgpr_read_b32 v206, a72\n\t"
      "v_mfma_f32_16x16x32_bf16 a[12:15], v[108:111], v[4:7], a[12:15]\n\t"
      "v_accvgpr_read_b32 v207, a88\n\t"
      "v_accvgpr_read_b32 v208, a104\n\t"
      "v_accvgpr_read_b32 v209, a120\n\t"
      "v_mul_f32 v202, v202, v170\n\t"
      "v_mul_f32 v203, v203, v170\n\t"
      "v_mfma_f32_16x16x32_bf16 a[28:31], v[108:111], v[12:15], a[28:31]\n\t"
      "v_mul_f32 v204, v204, v170\n\t"
      "v_mul_f32 v205, v205, v170\n\t"
      "v_mul_f32 v206, v206, v170\n\t"
      "v_mul_f32 v207, v207, v170\n\t"
      "v_mul_f32 v208, v208, v170\n\t"
      "v_mul_f32 v209, v209, v170\n\t"
      "v_mfma_f32_16x16x32_bf16 a[28:31], v[104:107], v[8:11], a[28:31]\n\t"
      "v_cvt_pk_bf16_f32 v210, v202, v203\n\t"
      "v_cvt_pk_bf16_f32 v211, v204, v205\n\t"
      "v_cvt_pk_bf16_f32 v212, v206, v207\n\t"
      "v_cvt_pk_bf16_f32 v213, v208, v209\n\t"
      "buffer_store_dwordx4 v[210:213], v242, s[72:75], s88 offen offset:0 sc1\n\t"
      "v_mfma_f32_16x16x32_bf16 a[44:47], v[104:107], v[16:19], a[44:47]\n\t"
      "v_accvgpr_read_b32 v178, a9\n\t"
      "v_accvgpr_read_b32 v179, a25\n\t"
      "v_accvgpr_read_b32 v180, a41\n\t"
      "v_accvgpr_read_b32 v181, a57\n\t"
      "v_accvgpr_read_b32 v182, a73\n\t"
      "v_mfma_f32_16x16x32_bf16 a[44:47], v[108:111], v[20:23], a[44:47]\n\t"
      "v_accvgpr_read_b32 v183, a89\n\t"
      "v_accvgpr_read_b32 v184, a105\n\t"
      "v_accvgpr_read_b32 v185, a121\n\t"
      "v_mul_f32 v178, v178, v172\n\t"
      "v_mul_f32 v179, v179, v172\n\t"
      "v_mfma_f32_16x16x32_bf16 a[60:63], v[108:111], v[28:31], a[60:63]\n\t"
      "v_mul_f32 v180, v180, v172\n\t"
      "v_mul_f32 v181, v181, v172\n\t"
      "v_mul_f32 v182, v182, v172\n\t"
      "v_mul_f32 v183, v183, v172\n\t"
      "v_mul_f32 v184, v184, v172\n\t"
      "v_mul_f32 v185, v185, v172\n\t"
      "v_mfma_f32_16x16x32_bf16 a[60:63], v[104:107], v[24:27], a[60:63]\n\t"
      "v_cvt_pk_bf16_f32 v186, v178, v179\n\t"
      "v_cvt_pk_bf16_f32 v187, v180, v181\n\t"
      "v_cvt_pk_bf16_f32 v188, v182, v183\n\t"
      "v_cvt_pk_bf16_f32 v189, v184, v185\n\t"
      "buffer_store_dwordx4 v[186:189], v242, s[72:75], s88 offen offset:256 sc1\n\t"
      "v_mfma_f32_16x16x32_bf16 a[76:79], v[104:107], v[32:35], a[76:79]\n\t"
      "v_accvgpr_read_b32 v190, a10\n\t"
      "v_accvgpr_read_b32 v191, a26\n\t"
      "v_accvgpr_read_b32 v192, a42\n\t"
      "v_accvgpr_read_b32 v193, a58\n\t"
      "v_accvgpr_read_b32 v194, a74\n\t"
      "v_mfma_f32_16x16x32_bf16 a[76:79], v[108:111], v[36:39], a[76:79]\n\t"
      "v_accvgpr_read_b32 v195, a90\n\t"
      "v_accvgpr_read_b32 v196, a106\n\t"
      "v_accvgpr_read_b32 v197, a122\n\t"
      "v_mul_f32 v190, v190, v174\n\t"
      "v_mul_f32 v191, v191, v174\n\t"
      "v_mfma_f32_16x16x32_bf16 a[92:95], v[108:111], v[44:47], a[92:95]\n\t"
      "v_mul_f32 v192, v192, v174\n\t"
      "v_mul_f32 v193, v193, v174\n\t"
      "v_mul_f32 v194, v194, v174\n\t"
      "v_mul_f32 v195, v195, v174\n\t"
      "v_mul_f32 v196, v196, v174\n\t"
      "v_mfma_f32_16x16x32_bf16 a[92:95], v[104:107], v[40:43], a[92:95]\n\t"
      "v_mul_f32 v197, v197, v174\n\t"
      "v_cvt_pk_bf16_f32 v198, v190, v191\n\t"
      "v_cvt_pk_bf16_f32 v199, v192, v193\n\t"
      "v_cvt_pk_bf16_f32 v200, v194, v195\n\t"
      "v_cvt_pk_bf16_f32 v201, v196, v197\n\t"
      "buffer_store_dwordx4 v[198:201], v242, s[72:75], s88 offen offset:512 sc1\n\t"
      "v_mfma_f32_16x16x32_bf16 a[108:111], v[104:107], v[48:51], a[108:111]\n\t"
      "v_accvgpr_read_b32 v202, a11\n\t"
      "v_accvgpr_read_b32 v203, a27\n\t"
      "v_accvgpr_read_b32 v204, a43\n\t"
      "v_accvgpr_read_b32 v205, a59\n\t"
      "v_accvgpr_read_b32 v206, a75\n\t"
      "v_mfma_f32_16x16x32_bf16 a[108:111], v[108:111], v[52:55], a[108:111]\n\t"
      "v_accvgpr_read_b32 v207, a91\n\t"
      "v_accvgpr_read_b32 v208, a107\n\t"
      "v_accvgpr_read_b32 v209, a123\n\t"
      "v_mul_f32 v202, v202, v176\n\t"
      "v_mul_f32 v203, v203, v176\n\t"
      "v_mfma_f32_16x16x32_bf16 a[124:127], v[108:111], v[60:63], a[124:127]\n\t"
      "v_mul_f32 v204, v204, v176\n\t"
      "v_mul_f32 v205, v205, v176\n\t"
      "v_mul_f32 v206, v206, v176\n\t"
      "v_mul_f32 v207, v207, v176\n\t"
      "v_mul_f32 v208, v208, v176\n\t"
      "v_mfma_f32_16x16x32_bf16 a[124:127], v[104:107], v[56:59], a[124:127]\n\t"
      "v_mul_f32 v209, v209, v176\n\t"
      "v_cvt_pk_bf16_f32 v210, v202, v203\n\t"
      "v_cvt_pk_bf16_f32 v211, v204, v205\n\t"
      "v_cvt_pk_bf16_f32 v212, v206, v207\n\t"
      "v_cvt_pk_bf16_f32 v213, v208, v209\n\t"
      "buffer_store_dwordx4 v[210:213], v242, s[72:75], s88 offen offset:768 sc1\n\t"
      "v_mov_b32_dpp v148, v148 row_newbcast:0 row_mask:0xf bank_mask:0xf\n\t"
      "v_mov_b32_dpp v149, v149 row_newbcast:0 row_mask:0xf bank_mask:0xf\n\t"
      "v_mov_b32_dpp v150, v150 row_newbcast:0 row_mask:0xf bank_mask:0xf\n\t"
      "v_mov_b32_dpp v151, v151 row_newbcast:0 row_mask:0xf bank_mask:0xf\n\t"
      "v_rcp_f32 v170, v148\n\t"
      "v_rcp_f32 v172, v149\n\t"
      "v_rcp_f32 v174, v150\n\t"
      "v_rcp_f32 v176, v151\n\t"
      "s_add_u32 s88, s97, 12288\n\t"
      "s_and_b32 s88, s88, 0x3fff\n\t"
      "v_accvgpr_read_b32 v178, a12\n\t"
      "v_accvgpr_read_b32 v179, a28\n\t"
      "v_accvgpr_read_b32 v180, a44\n\t"
      "v_accvgpr_read_b32 v181, a60\n\t"
      "v_accvgpr_read_b32 v182, a76\n\t"
      "v_accvgpr_read_b32 v183, a92\n\t"
      "v_accvgpr_read_b32 v184, a108\n\t"
      "v_accvgpr_read_b32 v185, a124\n\t"
      "v_pk_mul_f32 v[178:179], v[178:179], v[170:171] op_sel_hi:[1,0]\n\t"
      "v_pk_mul_f32 v[180:181], v[180:181], v[170:171] op_sel_hi:[1,0]\n\t"
      "v_pk_mul_f32 v[182:183], v[182:183], v[170:171] op_sel_hi:[1,0]\n\t"
      "v_pk_mul_f32 v[184:185], v[184:185], v[170:171] op_sel_hi:[1,0]\n\t"
      "v_cvt_pk_bf16_f32 v186, v178, v179\n\t"
      "v_cvt_pk_bf16_f32 v187, v180, v181\n\t"
      "v_cvt_pk_bf16_f32 v188, v182, v183\n\t"
      "v_cvt_pk_bf16_f32 v189, v184, v185\n\t"
      "buffer_store_dwordx4 v[186:189], v242, s[72:75], s88 offen offset:0 sc1\n\t"
      "v_accvgpr_read_b32 v190, a13\n\t"
      "v_accvgpr_read_b32 v191, a29\n\t"
      "v_accvgpr_read_b32 v192, a45\n\t"
      "v_accvgpr_read_b32 v193, a61\n\t"
      "v_accvgpr_read_b32 v194, a77\n\t"
      "v_accvgpr_read_b32 v195, a93\n\t"
      "v_accvgpr_read_b32 v196, a109\n\t"
      "v_accvgpr_read_b32 v197, a125\n\t"
      "v_pk_mul_f32 v[190:191], v[190:191], v[172:173] op_sel_hi:[1,0]\n\t"
      "v_pk_mul_f32 v[192:193], v[192:193], v[172:173] op_sel_hi:[1,0]\n\t"
      "v_pk_mul_f32 v[194:195], v[194:195], v[172:173] op_sel_hi:[1,0]\n\t"
      "v_pk_mul_f32 v[196:197], v[196:197], v[172:173] op_sel_hi:[1,0]\n\t"
      "v_cvt_pk_bf16_f32 v198, v190, v191\n\t"
      "v_cvt_pk_bf16_f32 v199, v192, v193\n\t"
      "v_cvt_pk_bf16_f32 v200, v194, v195\n\t"
      "v_cvt_pk_bf16_f32 v201, v196, v197\n\t"
      "buffer_store_dwordx4 v[198:201], v242, s[72:75], s88 offen offset:256 sc1\n\t"
      "v_accvgpr_read_b32 v202, a14\n\t"
      "v_accvgpr_read_b32 v203, a30\n\t"
      "v_accvgpr_read_b32 v204, a46\n\t"
      "v_accvgpr_read_b32 v205, a62\n\t"
      "v_accvgpr_read_b32 v206, a78\n\t"
      "v_accvgpr_read_b32 v207, a94\n\t"
      "v_accvgpr_read_b32 v208, a110\n\t"
      "v_accvgpr_read_b32 v209, a126\n\t"
      "v_pk_mul_f32 v[202:203], v[202:203], v[174:175] op_sel_hi:[1,0]\n\t"
      "v_pk_mul_f32 v[204:205], v[204:205], v[174:175] op_sel_hi:[1,0]\n\t"
      "v_pk_mul_f32 v[206:207], v[206:207], v[174:175] op_sel_hi:[1,0]\n\t"
      "v_pk_mul_f32 v[208:209], v[208:209], v[174:175] op_sel_hi:[1,0]\n\t"
      "v_cvt_pk_bf16_f32 v210, v202, v203\n\t"
      "v_cvt_pk_bf16_f32 v211, v204, v205\n\t"
      "v_cvt_pk_bf16_f32 v212, v206, v207\n\t"
      "v_cvt_pk_bf16_f32 v213, v208, v209\n\t"
      "buffer_store_dwordx4 v[210:213], v242, s[72:75], s88 offen offset:512 sc1\n\t"
      "v_accvgpr_read_b32 v178, a15\n\t"
      "v_accvgpr_read_b32 v179, a31\n\t"
      "v_accvgpr_read_b32 v180, a47\n\t"
      "v_accvgpr_read_b32 v181, a63\n\t"
      "v_accvgpr_read_b32 v182, a79\n\t"
      "v_accvgpr_read_b32 v183, a95\n\t"
      "v_accvgpr_read_b32 v184, a111\n\t"
      "v_accvgpr_read_b32 v185, a127\n\t"
      "v_pk_mul_f32 v[178:179], v[178:179], v[176:177] op_sel_hi:[1,0]\n\t"
      "v_pk_mul_f32 v[180:181], v[180:181], v[176:177] op_sel_hi:[1,0]\n\t"
      "v_pk_mul_f32 v[182:183], v[182:183], v[176:177] op_sel_hi:[1,0]\n\t"
      "v_pk_mul_f32 v[184:185], v[184:185], v[176:177] op_sel_hi:[1,0]\n\t"
      "v_cvt_pk_bf16_f32 v186, v178, v179\n\t"
      "v_cvt_pk_bf16_f32 v187, v180, v181\n\t"
      "v_cvt_pk_bf16_f32 v188, v182, v183\n\t"
      "v_cvt_pk_bf16_f32 v189, v184, v185\n\t"
      "buffer_store_dwordx4 v[186:189], v242, s[72:75], s88 offen offset:768 sc1\n\t"
      "ds_read_b32 v225, v224\n\t"
      "v_add_u32 v163, %2, v240\n\t"
      "v_min_u32 v163, 1, v163\n\t"
      "v_lshlrev_b32 v163, 24, v163\n\t"
      "s_lshl_b32 s94, %5, 3\n\t"
      "buffer_store_dwordx2 v[168:169], v163, s[36:39], s94 offen sc1\n\t"
      "s_mov_b32 s87, 0x71800000\n\t"
      "s_mov_b32 s88, 0x21800000\n\t"
      "s_waitcnt lgkmcnt(0)\n\t"
      "v_cmp_ne_u32 vcc, 0, v225\n\t"
      "s_nop 1\n\t"
      "s_mov_b64 s[76:77], vcc\n\t"
      "v_cmp_nge_f32 vcc, s87, v128\n\t"
      "v_cmp_nle_f32 s[78:79], s88, v128\n\t"
      "s_nop 0\n\t"
      "s_or_b64 s[76:77], s[76:77], vcc\n\t"
      "s_or_b64 s[76:77], s[76:77], s[78:79]\n\t"
      "v_cmp_nge_f32 vcc, s87, v129\n\t"
      "v_cmp_nle_f32 s[78:79], s88, v129\n\t"
      "s_nop 0\n\t"
      "s_or_b64 s[76:77], s[76:77], vcc\n\t"
      "s_or_b64 s[76:77], s[76:77], s[78:79]\n\t"
      "v_cmp_nge_f32 vcc, s87, v130\n\t"
      "v_cmp_nle_f32 s[78:79], s88, v130\n\t"
      "s_nop 0\n\t"
      "s_or_b64 s[76:77], s[76:77], vcc\n\t"
      "s_or_b64 s[76:77], s[76:77], s[78:79]\n\t"
      "v_cmp_nge_f32 vcc, s87, v131\n\t"
      "v_cmp_nle_f32 s[78:79], s88, v131\n\t"
      "s_nop 0\n\t"
      "s_or_b64 s[76:77], s[76:77], vcc\n\t"
      "s_or_b64 s[76:77], s[76:77], s[78:79]\n\t"
      "v_cmp_nge_f32 vcc, s87, v132\n\t"
      "v_cmp_nle_f32 s[78:79], s88, v132\n\t"
      "s_nop 0\n\t"
      "s_or_b64 s[76:77], s[76:77], vcc\n\t"
      "s_or_b64 s[76:77], s[76:77], s[78:79]\n\t"
      "v_cmp_nge_f32 vcc, s87, v133\n\t"
      "v_cmp_nle_f32 s[78:79], s88, v133\n\t"
      "s_nop 0\n\t"
      "s_or_b64 s[76:77], s[76:77], vcc\n\t"
      "s_or_b64 s[76:77], s[76:77], s[78:79]\n\t"
      "v_cmp_nge_f32 vcc, s87, v134\n\t"
      "v_cmp_nle_f32 s[78:79], s88, v134\n\t"
      "s_nop 0\n\t"
      "s_or_b64 s[76:77], s[76:77], vcc\n\t"
      "s_or_b64 s[76:77], s[76:77], s[78:79]\n\t"
      "v_cmp_nge_f32 vcc, s87, v135\n\t"
      "v_cmp_nle_f32 s[78:79], s88, v135\n\t"
      "s_nop 0\n\t"
      "s_or_b64 s[76:77], s[76:77], vcc\n\t"
      "s_or_b64 s[76:77], s[76:77], s[78:79]\n\t"
      "v_cmp_nge_f32 vcc, s87, v140\n\t"
      "v_cmp_nle_f32 s[78:79], s88, v140\n\t"
      "s_nop 0\n\t"
      "s_or_b64 s[76:77], s[76:77], vcc\n\t"
      "s_or_b64 s[76:77], s[76:77], s[78:79]\n\t"
      "v_cmp_nge_f32 vcc, s87, v141\n\t"
      "v_cmp_nle_f32 s[78:79], s88, v141\n\t"
      "s_nop 0\n\t"
      "s_or_b64 s[76:77], s[76:77], vcc\n\t"
      "s_or_b64 s[76:77], s[76:77], s[78:79]\n\t"
      "v_cmp_nge_f32 vcc, s87, v142\n\t"
      "v_cmp_nle_f32 s[78:79], s88, v142\n\t"
      "s_nop 0\n\t"
      "s_or_b64 s[76:77], s[76:77], vcc\n\t"
      "s_or_b64 s[76:77], s[76:77], s[78:79]\n\t"
      "v_cmp_nge_f32 vcc, s87, v143\n\t"
      "v_cmp_nle_f32 s[78:79], s88, v143\n\t"
      "s_nop 0\n\t"
      "s_or_b64 s[76:77], s[76:77], vcc\n\t"
      "s_or_b64 s[76:77], s[76:77], s[78:79]\n\t"
      "v_cmp_nge_f32 vcc, s87, v148\n\t"
      "v_cmp_nle_f32 s[78:79], s88, v148\n\t"
      "s_nop 0\n\t"
      "s_or_b64 s[76:77], s[76:77], vcc\n\t"
      "s_or_b64 s[76:77], s[76:77], s[78:79]\n\t"
      "v_cmp_nge_f32 vcc, s87, v149\n\t"
      "v_cmp_nle_f32 s[78:79], s88, v149\n\t"
      "s_nop 0\n\t"
      "s_or_b64 s[76:77], s[76:77], vcc\n\t"
      "s_or_b64 s[76:77], s[76:77], s[78:79]\n\t"
      "v_cmp_nge_f32 vcc, s87, v150\n\t"
      "v_cmp_nle_f32 s[78:79], s88, v150\n\t"
      "s_nop 0\n\t"
      "s_or_b64 s[76:77], s[76:77], vcc\n\t"
      "s_or_b64 s[76:77], s[76:77], s[78:79]\n\t"
      "v_cmp_nge_f32 vcc, s87, v151\n\t"
      "v_cmp_nle_f32 s[78:79], s88, v151\n\t"
      "s_nop 0\n\t"
      "s_or_b64 s[76:77], s[76:77], vcc\n\t"
      "s_or_b64 s[76:77], s[76:77], s[78:79]\n\t"
      "s_waitcnt vmcnt(0)\n\t"
      "s_or_b32 %0, s76, s77\n\t"
      "s_branch .Lattn_end\n\t"
      ".Lspin_pro:\n\t"
      "s_cmp_eq_u32 s96, 0\n\t"
      "s_cbranch_scc1 .Lgiveup_pro\n\t"
      "s_sub_u32 s96, s96, 1\n\t"
      "s_sleep 2\n\t"
      "buffer_load_dwordx2 v[166:167], v162, s[36:39], s30 offen sc1\n\t"
      "s_waitcnt vmcnt(0)\n\t"
      "v_cmp_ne_u64 vcc, v[166:167], v[164:165]\n\t"
      "s_nop 1\n\t"
      "s_cbranch_vccnz .Lspin_pro\n\t"
      "s_branch .Lback_pro\n\t"
      ".Lgiveup_pro:\n\t"
      "v_mov_b32 v166, %3\n\t"
      "v_add_u32 v166, 147456, v166\n\t"
      "v_mov_b32 v167, 1\n\t"
      "ds_write_b32 v166, v167\n\t"
      "s_waitcnt lgkmcnt(0)\n\t"
      "s_branch .Lback_pro\n\t"
      ".Lspin_loop:\n\t"
      "s_cmp_eq_u32 s96, 0\n\t"
      "s_cbranch_scc1 .Lgiveup_loop\n\t"
      "s_sub_u32 s96, s96, 1\n\t"
      "s_sleep 2\n\t"
      "buffer_load_dwordx2 v[166:167], v162, s[36:39], s94 offen sc1\n\t"
      "s_waitcnt vmcnt(0)\n\t"
      "v_cmp_ne_u64 vcc, v[166:167], v[164:165]\n\t"
      "s_nop 1\n\t"
      "s_cbranch_vccnz .Lspin_loop\n\t"
      "s_branch .Lback_loop\n\t"
      ".Lgiveup_loop:\n\t"
      "v_mov_b32 v166, %3\n\t"
      "v_add_u32 v166, 147456, v166\n\t"
      "v_mov_b32 v167, 1\n\t"
      "ds_write_b32 v166, v167\n\t"
      "s_waitcnt lgkmcnt(0)\n\t"
      "s_branch .Lback_loop\n\t"
      ".Lattn_end:\n\t"
      : "=&s"(fail)
      : "s"(__builtin_amdgcn_kernarg_segment_ptr()), "s"(wid), "s"(ldsb), "s"(h), "s"(j)
      : "v0","v1","v2","v3","v4","v5","v6","v7","v8","v9","v10","v11","v12","v13","v14","v15","v16","v17","v18","v19","v20","v21","v22","v23","v24","v25","v26","v27","v28","v29","v30","v31","v32","v33","v34","v35","v36","v37","v38","v39","v40","v41","v42","v43","v44","v45","v46","v47","v48","v49","v50","v51","v52","v53","v54","v55","v56","v57","v58","v59","v60","v61","v62","v63","v64","v65","v66","v67","v68","v69","v70","v71","v72","v73","v74","v75","v76","v77","v78","v79","v80","v81","v82","v83","v84","v85","v86","v87","v88","v89","v90","v91","v92","v93","v94","v95","v96","v97","v98","v99","v100","v101","v102","v103","v104","v105","v106","v107","v108","v109","v110","v111","v112","v113","v114","v115","v116","v117","v118","v119","v120","v121","v122","v123","v124","v125","v126","v127","v128","v129","v130","v131","v132","v133","v134","v135","v136","v137","v138","v139","v140","v141","v142","v143","v144","v145","v146","v147","v148","v149","v150","v151","v152","v153","v154","v155","v156","v157","v158","v159","v160","v161","v162","v163","v164","v165","v166","v167","v168","v169","v170","v171","v172","v173","v174","v175","v176","v177","v178","v179","v180","v181","v182","v183","v184","v185","v186","v187","v188","v189","v190","v191","v192","v193","v194","v195","v196","v197","v198","v199","v200","v201","v202","v203","v204","v205","v206","v207","v208","v209","v210","v211","v212","v213","v214","v215","v216","v217","v218","v219","v220","v221","v222","v223","v224","v225","v226","v227","v228","v229","v230","v231","v232","v233","v234","v235","v236","v237","v238","v239","v240","v241","v242","v243","v244","v245","v246","v247","v248","v249","v250","v251","v252","v253","a0","a1","a2","a3","a4","a5","a6","a7","a8","a9","a10","a11","a12","a13","a14","a15","a16","a17","a18","a19","a20","a21","a22","a23","a24","a25","a26","a27","a28","a29","a30","a31","a32","a33","a34","a35","a36","a37","a38","a39","a40","a41","a42","a43","a44","a45","a46","a47","a48","a49","a50","a51","a52","a53","a54","a55","a56","a57","a58","a59","a60","a61","a62","a63","a64","a65","a66","a67","a68","a69","a70","a71","a72","a73","a74","a75","a76","a77","a78","a79","a80","a81","a82","a83","a84","a85","a86","a87","a88","a89","a90","a91","a92","a93","a94","a95","a96","a97","a98","a99","a100","a101","a102","a103","a104","a105","a106","a107","a108","a109","a110","a111","a112","a113","a114","a115","a116","a117","a118","a119","a120","a121","a122","a123","a124","a125","a126","a127","a128","a129","a130","a131","a132","a133","a134","a135","a136","a137","a138","a139","a140","a141","a142","a143","a144","a145","a146","a147","a148","a149","a150","a151","a152","a153","a154","a155","a156","a157","a158","a159","a160","a161","a162","a163","a164","a165","a166","a167","a168","a169","a170","a171","a172","a173","a174","a175","a176","a177","a178","a179","a180","a181","a182","a183","a184","a185","a186","a187","a188","a189","a190","a191","a192","a193","a194","a195","a196","a197","a198","a199","a200","a201","a202","a203","a204","a205","a206","a207","a208","a209","a210","a211","a212","a213","a214","a215","a216","a217","a218","a219","a220","a221","a222","a223","a224","a225","a226","a227","a228","a229","a230","a231","a232","a233","a234","a235","a236","a237","a238","a239","a240","a241","a242","a243","a244","a245","a246","a247","a248","a249","a250","a251","a252","a253","a254","a255","s36","s37","s38","s39","s40","s41","s42","s43","s44","s45","s46","s47","s48","s49","s50","s51","s52","s53","s54","s55","s56","s57","s58","s59","s60","s61","s62","s63","s64","s65","s66","s67","s68","s69","s70","s71","s72","s73","s74","s75","s76","s77","s78","s79","s80","s81","s82","s83","s84","s85","s86","s87","s88","s89","s90","s91","s92","s93","s94","s95","s96","s97","s98","s99","s24","s25","s26","s27","s28","s29","s30","s31","vcc","scc","memory");
  if (fail) slow_path(a, h, 256 * j + 64 * wid);
}

extern "C" void kernel_launch(void* const* d_in, const int* in_sizes, int n_in, void* d_out, int out_size, void* d_ws, size_t ws_size, hipStream_t stream) {
  (void)hipFuncSetAttribute((const void*)attn, hipFuncAttributeMaxDynamicSharedMemorySize, LDS_BYTES);
  Args a;
  a.q = (const u16*)d_in[0]; a.qk = (const int*)d_in[1]; a.ksc = (const u16*)d_in[2]; a.kzp = (const u16*)d_in[3];
  a.qv = (const int*)d_in[4]; a.vsc = (const u16*)d_in[5]; a.vzp = (const u16*)d_in[6];
  a.qc = (const u16*)d_in[7]; a.qs = (const u16*)d_in[8]; a.kc = (const u16*)d_in[9]; a.ks = (const u16*)d_in[10];
  a.out = (u16*)d_out; a.ws = (char*)d_ws;
  hipLaunchKernelGGL(attn, dim3(256), dim3(256), LDS_BYTES, stream, a);
}
```
